```python
import torch

def _softmax(x, axis=-1):
    return torch.softmax(x, dim=axis)
def _gelu(x):
    return torch.nn.functional.gelu(x, approximate="tanh")
def _silu(x):
    return torch.nn.functional.silu(x)
def _sigmoid(x):
    return torch.sigmoid(x)
def _layernorm(x, axis=-1, eps=1e-5):
    m = x.mean(dim=axis, keepdim=True)
    v = x.var(dim=axis, keepdim=True, unbiased=False)
    return (x - m) / torch.sqrt(v + eps)
def _scatter_add(tgt, idx, src, axis=0):
    return tgt.clone().index_add_(axis, idx, src)
def _segment_sum(src, seg, n):
    out = torch.zeros((n,) + src.shape[1:], dtype=src.dtype)
    return out.index_add_(0, seg, src)
def _pad(x, pw):
    flat = tuple(v for pair in reversed(pw) for v in pair)
    return torch.nn.functional.pad(x, flat)

kernel_name = 'gated_mix__compressed_kv_cla__geglu_ffn__decode'

def setup_inputs():

    _w = (lambda a: (lambda t: t.to(torch.bfloat16) if t.is_floating_point() else t)(torch.as_tensor(a)))
    def _A_setup():
        B, H, Sq, Sk, D, M = 2, 32, 1, 8192, 128, 512
        Dc = 4 * D
        return {
            "x": _w((lambda *s: torch.randn(*s, dtype=torch.bfloat16) * 0.05)(B, Sq, M)),
            "c_kv": _w((lambda *s: torch.randn(*s, dtype=torch.bfloat16) * 1.6)(B, Sk, Dc)),
            "w_q": _w((lambda *s: torch.randn(*s, dtype=torch.bfloat16) * 6.4)(M, H, D) * (M ** -0.5)),
            "w_uk": _w((lambda *s: torch.randn(*s, dtype=torch.bfloat16) * 6.4)(Dc, H, D) * (Dc ** -0.5)),
            "w_uv": _w((lambda *s: torch.randn(*s, dtype=torch.bfloat16) * 1.6)(Dc, H, D) * (Dc ** -0.5)),
            "w_o": _w((lambda *s: torch.randn(*s, dtype=torch.bfloat16) * 1.6)(H, D, M) * ((H * D) ** -0.5)),
        }
    _A_in = _A_setup()

    def _B_setup():
        B, S, D, F = 2, 1, 512, 1792
        return {
            "x": _w((lambda *s: torch.randn(*s, dtype=torch.bfloat16) * 0.05)(B, S, D)),
            "w_gate": _w((lambda *s: torch.randn(*s, dtype=torch.bfloat16) * 0.4)(D, F)),
            "w_up": _w((lambda *s: torch.randn(*s, dtype=torch.bfloat16) * 0.4)(D, F)),
            "w_down": _w((lambda *s: torch.randn(*s, dtype=torch.bfloat16) * 0.4)(F, D)),
        }
    _B_in = _B_setup()

    return {
        "A_x": _A_in["x"],
        "A_c_kv": _A_in["c_kv"],
        "A_w_q": _A_in["w_q"],
        "A_w_uk": _A_in["w_uk"],
        "A_w_uv": _A_in["w_uv"],
        "A_w_o": _A_in["w_o"],
        "B_x": _B_in["x"],
        "B_w_gate": _B_in["w_gate"],
        "B_w_up": _B_in["w_up"],
        "B_w_down": _B_in["w_down"],
        "gate_w": _w((lambda *s: torch.randn(*s, dtype=torch.bfloat16) * 0.1)(512) * 16.0),
    }

def reference(A_x, A_c_kv, A_w_q, A_w_uk, A_w_uv, A_w_o, B_x, B_w_gate, B_w_up, B_w_down, gate_w):
    def _A_ref(x, c_kv, w_q, w_uk, w_uv, w_o):
        B, Sq, M = x.shape[0], x.shape[1], x.shape[2]
        Sk = c_kv.shape[1]
        H, D = w_q.shape[1], w_q.shape[2]
        q = torch.einsum("bqm,mhd->bhqd", x, w_q)
        k = torch.einsum("bkc,chd->bhkd", c_kv, w_uk)
        v = torch.einsum("bkc,chd->bhkd", c_kv, w_uv)
        scores = torch.einsum("bhqd,bhkd->bhqk", q, k) * (D ** -0.5)
        off = Sk - Sq
        qi = torch.arange(Sq)[:, None]
        kj = torch.arange(Sk)[None, :]
        cmask = kj <= (qi + off)
        scores = torch.where(cmask[None, None], scores, -1e9)
        p = _softmax(scores, axis=-1)
        ctx = torch.einsum("bhqk,bhkd->bhqd", p, v)
        return torch.einsum("bhqd,hdm->bqm", ctx, w_o)

    def _B_ref(x, w_gate, w_up, w_down):
        return (_gelu(x @ w_gate) * (x @ w_up)) @ w_down

    x = A_x
    g = _sigmoid((x * gate_w).sum(-1, keepdims=True))
    a = _A_ref(x, A_c_kv, A_w_q, A_w_uk, A_w_uv, A_w_o)
    b = _B_ref(x, B_w_gate, B_w_up, B_w_down)
    return g * a + (1 - g) * b
```

```cpp
#include <hip/hip_runtime.h>
#include <hip/hip_bf16.h>
#include <stdint.h>

typedef unsigned short u16;
__device__ __forceinline__ float bf2f(u16 v){ return __uint_as_float(((unsigned)v)<<16); }
__device__ __forceinline__ u16 f2bf(float f){ __hip_bfloat16 h = __float2bfloat16(f); return *reinterpret_cast<u16*>(&h); }

constexpr int B=2,H=32,SK=8192,D=128,M=512,DC=512,F=1792;
constexpr int NSPLIT=64, KPS=SK/NSPLIT;

__global__ void k_q(const u16* x,const u16* wq,float* q){
  int col = blockIdx.x*blockDim.x+threadIdx.x;
  float a0=0,a1=0;
  for(int m=0;m<M;m++){ float w=bf2f(wq[(size_t)m*4096+col]); a0+=bf2f(x[m])*w; a1+=bf2f(x[M+m])*w; }
  q[col]=a0; q[4096+col]=a1;
}
__global__ void k_qabs(const float* q,const u16* wuk,float* qa){
  int idx = blockIdx.x*blockDim.x+threadIdx.x;
  int h=idx>>9,c=idx&511;
  float a0=0,a1=0;
  for(int d=0;d<D;d++){ float w=bf2f(wuk[((size_t)c*H+h)*D+d]); a0+=q[h*D+d]*w; a1+=q[4096+h*D+d]*w; }
  const float sc=0.08838834764831845f;
  qa[idx]=a0*sc; qa[16384+idx]=a1*sc;
}
__global__ __launch_bounds__(128) void k_attn(const u16* ckv,const float* qa,float* opart,float* ml){
  int h=blockIdx.x, s=blockIdx.y, b=blockIdx.z;
  __shared__ float sq[512]; __shared__ float sp[KPS]; __shared__ float red[128];
  int t=threadIdx.x;
  for(int i=t;i<512;i+=128) sq[i]=qa[(b*H+h)*512+i];
  __syncthreads();
  const u16* base=ckv+((size_t)b*SK+s*KPS)*DC;
  float acc=0; { const u16* r=base+(size_t)t*DC; for(int c=0;c<DC;c++) acc+=bf2f(r[c])*sq[c]; }
  red[t]=acc; __syncthreads();
  float mx=-1e30f; for(int i=0;i<128;i++) mx=fmaxf(mx,red[i]);
  float p=__expf(acc-mx); sp[t]=p; __syncthreads();
  float l=0; for(int i=0;i<128;i++) l+=sp[i];
  for(int c=t;c<DC;c+=128){ float o=0; for(int k=0;k<KPS;k++) o+=sp[k]*bf2f(base[(size_t)k*DC+c]); opart[(((size_t)b*H+h)*NSPLIT+s)*DC+c]=o; }
  if(t==0){ ml[((b*H+h)*NSPLIT+s)*2]=mx; ml[((b*H+h)*NSPLIT+s)*2+1]=l; }
}
__global__ __launch_bounds__(128) void k_comb(const float* opart,const float* ml,const u16* wuv,float* ctx){
  int h=blockIdx.x,b=blockIdx.y,t=threadIdx.x;
  __shared__ float w[NSPLIT]; __shared__ float ol[512];
  float mx=-1e30f; for(int s=0;s<NSPLIT;s++) mx=fmaxf(mx,ml[((b*H+h)*NSPLIT+s)*2]);
  float L=0; for(int s=0;s<NSPLIT;s++) L+=ml[((b*H+h)*NSPLIT+s)*2+1]*__expf(ml[((b*H+h)*NSPLIT+s)*2]-mx);
  if(t<NSPLIT) w[t]=__expf(ml[((b*H+h)*NSPLIT+t)*2]-mx)/L;
  __syncthreads();
  for(int c=t;c<512;c+=128){ float o=0; for(int s=0;s<NSPLIT;s++) o+=w[s]*opart[(((size_t)b*H+h)*NSPLIT+s)*DC+c]; ol[c]=o; }
  __syncthreads();
  float a=0; for(int c=0;c<512;c++) a+=ol[c]*bf2f(wuv[((size_t)c*H+h)*D+t]);
  ctx[b*4096+h*D+t]=a;
}
__global__ void k_ffn1(const u16* x,const u16* wg,const u16* wu,float* hh){
  int f=blockIdx.x*blockDim.x+threadIdx.x; if(f>=F) return;
  float g0=0,g1=0,u0=0,u1=0;
  for(int m=0;m<M;m++){ float a=bf2f(wg[(size_t)m*F+f]),c=bf2f(wu[(size_t)m*F+f]); float x0=bf2f(x[m]),x1=bf2f(x[M+m]); g0+=x0*a;g1+=x1*a;u0+=x0*c;u1+=x1*c; }
  auto gelu=[](float v){ return 0.5f*v*(1.f+tanhf(0.7978845608028654f*(v+0.044715f*v*v*v))); };
  hh[f]=gelu(g0)*u0; hh[F+f]=gelu(g1)*u1;
}
__global__ void k_out(const u16* x,const u16* gw,const float* ctx,const u16* wo,const float* hh,const u16* wd,u16* out){
  int m=blockIdx.x*blockDim.x+threadIdx.x; int b=blockIdx.y;
  float s=0; for(int i=0;i<M;i++) s+=bf2f(x[b*M+i])*bf2f(gw[i]);
  float g=1.f/(1.f+__expf(-s));
  float a=0; for(int i=0;i<4096;i++) a+=ctx[b*4096+i]*bf2f(wo[(size_t)i*M+m]);
  float bb=0; for(int i=0;i<F;i++) bb+=hh[b*F+i]*bf2f(wd[(size_t)i*M+m]);
  out[b*M+m]=f2bf(g*a+(1.f-g)*bb);
}

extern "C" void kernel_launch(void* const* d_in, const int* in_sizes, int n_in,
                              void* d_out, int out_size, void* d_ws, size_t ws_size,
                              hipStream_t stream) {
  const u16* x=(const u16*)d_in[0]; const u16* ckv=(const u16*)d_in[1]; const u16* wq=(const u16*)d_in[2];
  const u16* wuk=(const u16*)d_in[3]; const u16* wuv=(const u16*)d_in[4]; const u16* wo=(const u16*)d_in[5];
  const u16* wg=(const u16*)d_in[7]; const u16* wu=(const u16*)d_in[8]; const u16* wd=(const u16*)d_in[9]; const u16* gw=(const u16*)d_in[10];
  float* ws=(float*)d_ws;
  float* q=ws; float* qa=q+8192; float* ml=qa+32768; float* ctx=ml+2*32*64*2; float* hh=ctx+8192; float* opart=hh+4096;
  k_q<<<4096/64,64,0,stream>>>(x,wq,q);
  k_qabs<<<16384/64,64,0,stream>>>(q,wuk,qa);
  k_attn<<<dim3(H,NSPLIT,B),128,0,stream>>>(ckv,qa,opart,ml);
  k_comb<<<dim3(H,B),128,0,stream>>>(opart,ml,wuv,ctx);
  k_ffn1<<<F/64,64,0,stream>>>(x,wg,wu,hh);
  k_out<<<dim3(M/64,B),64,0,stream>>>(x,gw,ctx,wo,hh,wd,(u16*)d_out);
}
```

```cpp
#include <hip/hip_runtime.h>
#include <hip/hip_bf16.h>
#include <stdint.h>

typedef unsigned short u16;
typedef unsigned v4u __attribute__((ext_vector_type(4)));
typedef unsigned v2u __attribute__((ext_vector_type(2)));
typedef float f32x4 __attribute__((ext_vector_type(4)));
typedef short s16x4 __attribute__((ext_vector_type(4)));
typedef __bf16 bf16x8 __attribute__((ext_vector_type(8)));
#define LAS __attribute__((address_space(3)))
#define GAS __attribute__((address_space(1)))

namespace {
constexpr int PITCH = 1056;
constexpr int L_KV = 0;
constexpr int L_Q = 128 * PITCH;
constexpr int L_P = L_Q + 16 * PITCH;
constexpr int L_SM = L_P + 4608;
constexpr int L_OL = L_SM + 1024;
constexpr int L_CTX = L_OL + 1024;
constexpr int L_HB = L_CTX + 2048;
constexpr int L_END = L_HB + 64;
static_assert(L_END <= 160 * 1024, "LDS");

constexpr unsigned W_EPOCH = 0;
constexpr unsigned W_H2 = 4096;
constexpr unsigned W_HF = W_H2 + 2 * 32 * 512 * 4;
constexpr unsigned W_H3S = W_HF + 224 * 2048;
constexpr unsigned W_H4 = W_H3S + 32 * 2 * 64 * 8;
constexpr unsigned W_H3 = W_H4 + 256 * 256 * 16;
constexpr unsigned W_DBG = W_H3 + 32 * 8 * 2 * 64 * 64 * 4;
constexpr unsigned W_END = W_DBG + 256 * 2 * 16 * 8;
constexpr int SPIN_CAP = 1 << 16;

__device__ __forceinline__ f32x4 mfma(v4u a, v4u b, f32x4 c) {
  return __builtin_amdgcn_mfma_f32_16x16x32_bf16(__builtin_bit_cast(bf16x8, a), __builtin_bit_cast(bf16x8, b), c, 0, 0, 0);
}
__device__ __forceinline__ float bf_lo(unsigned w) { return __uint_as_float(w << 16); }
__device__ __forceinline__ float bf_hi(unsigned w) { return __uint_as_float(w & 0xffff0000u); }
__device__ __forceinline__ unsigned pk_bf16(float a, float b) {
  typedef __bf16 bf16x2 __attribute__((ext_vector_type(2)));
  typedef float f32x2 __attribute__((ext_vector_type(2)));
  f32x2 v = {a, b};
  bf16x2 r = __builtin_convertvector(v, bf16x2);
  return __builtin_bit_cast(unsigned, r);
}
__device__ __forceinline__ unsigned tagw(float v, unsigned tag) { return ((__float_as_uint(v) + 0x80u) & 0xffffff00u) | tag; }
__device__ __forceinline__ float untag(unsigned w) { return __uint_as_float(w & 0xffffff00u); }
__device__ __forceinline__ v4u tag4(f32x4 v, unsigned tag) { return v4u{tagw(v.x, tag), tagw(v.y, tag), tagw(v.z, tag), tagw(v.w, tag)}; }
__device__ __forceinline__ unsigned bad4(v4u w, unsigned tag) { return ((w.x ^ tag) | (w.y ^ tag) | (w.z ^ tag) | (w.w ^ tag)) & 0xffu; }
__device__ __forceinline__ float ror(float v, int) = delete;
template <int N> __device__ __forceinline__ float dpp_ror(float v) {
  return __int_as_float(__builtin_amdgcn_update_dpp(0, __float_as_int(v), 0x120 + N, 0xf, 0xf, false));
}
#define BAR()                                              \
  do {                                                     \
    __builtin_amdgcn_sched_barrier(0);                     \
    asm volatile("s_waitcnt lgkmcnt(0)" ::: "memory");     \
    __builtin_amdgcn_s_barrier();                          \
    asm volatile("" ::: "memory");                         \
    __builtin_amdgcn_sched_barrier(0);                     \
  } while (0)
#define LD128(off) (*(const LAS v4u*)(L + (off)))
#define LDF4(off) (*(const LAS f32x4*)(L + (off)))

#define STAMP(i)

__global__ __launch_bounds__(512, 2) void mk(const u16* __restrict__ x, const u16* __restrict__ ckv, const u16* __restrict__ wq,
                                             const u16* __restrict__ wuk, const u16* __restrict__ wuv, const u16* __restrict__ wo,
                                             const u16* __restrict__ wg, const u16* __restrict__ wu, const u16* __restrict__ wd,
                                             const u16* __restrict__ gw, u16* __restrict__ out, unsigned char* ws) {
  extern __shared__ __attribute__((aligned(16))) unsigned char lds[];
  LAS unsigned char* L = (LAS unsigned char*)lds;
  const int tid = threadIdx.x, lane = tid & 63, wave = __builtin_amdgcn_readfirstlane(tid >> 6);
  const int bid = blockIdx.x, xcd = bid & 7, slot = bid >> 3;
  const int h = xcd * 4 + (slot >> 3), j = slot & 7;
  const int ab = xcd >> 2, as = (xcd & 3) * 16 + (slot >> 1), hg = slot & 1;
  const int g = lane >> 4, r = lane & 15, jj = r & 7;
  const auto rs = __builtin_amdgcn_make_buffer_rsrc(ws, 0, W_END, 0x00020000);
  STAMP(0);
  int vzero = 0;
  asm volatile("" : "+v"(vzero));
  unsigned epoch;
  unsigned selE[4], selO[4];
#pragma unroll
  for (int k = 0; k < 4; ++k) {
    selE[k] = (k == (jj >> 1)) ? ((jj & 1) ? 0x01000c0cu : 0x0c0c0100u) : 0x0c0c0c0cu;
    selO[k] = (k == (jj >> 1)) ? ((jj & 1) ? 0x03020c0cu : 0x0c0c0302u) : 0x0c0c0c0cu;
  }
  auto buildA = [&](unsigned pw, int odd) -> v4u {
    v4u a;
    if (odd) { a.x = __builtin_amdgcn_perm(pw, pw, selO[0]); a.y = __builtin_amdgcn_perm(pw, pw, selO[1]); a.z = __builtin_amdgcn_perm(pw, pw, selO[2]); a.w = __builtin_amdgcn_perm(pw, pw, selO[3]); }
    else     { a.x = __builtin_amdgcn_perm(pw, pw, selE[0]); a.y = __builtin_amdgcn_perm(pw, pw, selE[1]); a.z = __builtin_amdgcn_perm(pw, pw, selE[2]); a.w = __builtin_amdgcn_perm(pw, pw, selE[3]); }
    return a;
  };
  unsigned tag = 0;
  v4u wov[34];
#define wvv wov
  float gate0 = 0.f, gate1 = 0.f;

  if (wave < 4) {
    const int w = wave;
    const v4u* xp = (const v4u*)(x + (r >> 3) * 512 + 128 * w + 32 * g);
    v4u xv[4];
#pragma unroll
    for (int i = 0; i < 4; ++i) xv[i] = xp[i];
    const u16* wqb = wq + (size_t)(128 * w + 32 * g) * 4096 + h * 128 + 8 * r;
    v4u wv[32];
#pragma unroll
    for (int i = 0; i < 32; ++i) wv[i] = *(const v4u*)(wqb + (size_t)i * 4096);
    const u16* wkb = wuk + ((size_t)(64 * j + 16 * w + r) * 32 + h) * 128 + 8 * g;
    v4u wk[4];
#pragma unroll
    for (int ks = 0; ks < 4; ++ks) wk[ks] = *(const v4u*)(wkb + 32 * ks);
    const v4u gx0 = *(const v4u*)(x + lane * 8), gx1 = *(const v4u*)(x + 512 + lane * 8), gwv = *(const v4u*)(gw + lane * 8);
    epoch = __builtin_amdgcn_raw_buffer_load_b32(rs, vzero + W_EPOCH, 0, 16);
    BAR();
    f32x4 acc = {0.f, 0.f, 0.f, 0.f};
#pragma unroll
    for (int i = 0; i < 32; ++i) acc = mfma(buildA(xv[i >> 3][(i >> 1) & 3], i & 1), wv[i], acc);
    STAMP(1);
    {
      const unsigned h01 = pk_bf16(acc.x, acc.y), h23 = pk_bf16(acc.z, acc.w);
      const unsigned l01 = pk_bf16(acc.x - bf_lo(h01), acc.y - bf_hi(h01)), l23 = pk_bf16(acc.z - bf_lo(h23), acc.w - bf_hi(h23));
      const int b = g >> 1, d0 = 8 * r + 4 * (g & 1);
      *(LAS v2u*)(L + L_P + (b + 4 * w) * 288 + d0 * 2) = v2u{h01, h23};
      *(LAS v2u*)(L + L_P + (b + 2 + 4 * w) * 288 + d0 * 2) = v2u{l01, l23};
    }
    {
      float s0 = 0.f, s1 = 0.f;
#pragma unroll
      for (int k = 0; k < 4; ++k) {
        s0 += bf_lo(gx0[k]) * bf_lo(gwv[k]) + bf_hi(gx0[k]) * bf_hi(gwv[k]);
        s1 += bf_lo(gx1[k]) * bf_lo(gwv[k]) + bf_hi(gx1[k]) * bf_hi(gwv[k]);
      }
#pragma unroll
      for (int o = 1; o < 64; o <<= 1) { s0 += __shfl_xor(s0, o); s1 += __shfl_xor(s1, o); }
      gate0 = 1.f / (1.f + __expf(-s0));
      gate1 = 1.f / (1.f + __expf(-s1));
    }
    BAR();
    f32x4 qa = {0.f, 0.f, 0.f, 0.f};
#pragma unroll
    for (int ks = 0; ks < 4; ++ks) qa = mfma(wk[ks], LD128(L_P + r * 288 + ks * 64 + g * 16), qa);
#pragma unroll
    for (int e = 0; e < 4; ++e) { float v = qa[e]; v += dpp_ror<8>(v); v += dpp_ror<4>(v); v += dpp_ror<2>(v); qa[e] = v * (0.08838834764831845f * 1.4426950408889634f); }
    tag = (epoch + 1u) & 0xffu;
    if (r < 2) __builtin_amdgcn_raw_buffer_store_b128(tag4(qa, tag), rs, W_H2 + ((r * 32 + h) * 512 + 64 * j + 16 * w + 4 * g) * 4, 0, 16);
    STAMP(2);
    {
      v4u v[8];
      const unsigned base = W_H2 + (unsigned)(ab * 32 + hg * 16) * 2048u + (unsigned)tid * 16u;
      for (int spin = 0; spin < SPIN_CAP; ++spin) {
        asm volatile("" ::: "memory");
        unsigned bad = 0;
#pragma unroll
        for (int i = 0; i < 8; ++i) v[i] = __builtin_amdgcn_raw_buffer_load_b128(rs, base + i * 4096, 0, 16);
#pragma unroll
        for (int i = 0; i < 8; ++i) bad |= bad4(v[i], tag);
        if (__all(bad == 0)) break;
        __builtin_amdgcn_s_sleep(2);
      }
#pragma unroll
      for (int i = 0; i < 8; ++i) {
        const int head = 2 * i + (tid >> 7), c = 4 * (tid & 127);
        *(LAS v2u*)(L + L_Q + head * PITCH + c * 2) = v2u{pk_bf16(untag(v[i].x), untag(v[i].y)), pk_bf16(untag(v[i].z), untag(v[i].w))};
      }
    }
    STAMP(3);
    {
      const u16* wvb = wuv + ((size_t)(64 * j + 16 * g) * 32 + h) * 128 + 8 * r;
#pragma unroll
      for (int i = 0; i < 16; ++i) wvv[i] = *(const v4u*)(wvb + (size_t)i * 4096);
    }
    BAR();
  } else {
    const int w = wave - 4;
    BAR();
    const u16* src = ckv + ((size_t)ab * 8192 + as * 128 + w * 32) * 512 + lane * 8;
#pragma unroll
    for (int i = 0; i < 32; ++i)
      __builtin_amdgcn_global_load_lds((const void*)(src + i * 512), (LAS void*)(L + L_KV + (w * 32 + i) * PITCH), 16, 0, 0);
    v4u fv[8]; v4u fx;
    const bool ffn1 = (w < 2) && (bid < 224);
    const int cb = bid % 14, rb = bid / 14;
    if (ffn1) {
      const u16* fb = (w == 0 ? wg : wu) + (size_t)(32 * rb + 8 * g) * 1792 + 128 * cb + 8 * r;
#pragma unroll
      for (int i = 0; i < 8; ++i) fv[i] = *(const v4u*)(fb + (size_t)i * 1792);
      fx = *(const v4u*)(x + (r >> 3) * 512 + 32 * rb + 8 * g);
    }
    epoch = __builtin_amdgcn_raw_buffer_load_b32(rs, vzero + W_EPOCH, 0, 16);
    BAR();
    __builtin_amdgcn_s_waitcnt(0x0f70);
    STAMP(1);
    tag = (epoch + 1u) & 0xffu;
    if (ffn1) {
      f32x4 acc = {0.f, 0.f, 0.f, 0.f};
#pragma unroll
      for (int i = 0; i < 8; ++i) acc = mfma(buildA(fx[i >> 1], i & 1), fv[i], acc);
      const unsigned u = 16 * cb + r;
      __builtin_amdgcn_raw_buffer_store_b128(tag4(acc, tag), rs, W_HF + u * 2048 + ((w * 16 + rb) * 4 + g) * 16, 0, 16);
    }
    STAMP(2);
    BAR();
    {
      const u16* wob = wo + ((size_t)h * 128 + 32 * g) * 512 + 128 * w + 8 * r;
#pragma unroll
      for (int i = 0; i < 32; ++i) wov[i] = *(const v4u*)(wob + i * 512);
      const int u = bid < 224 ? bid : 0;
      const u16* wdb = wd + (size_t)(8 * u + 2 * g) * 512 + 128 * w + 8 * r;
      wov[32] = *(const v4u*)(wdb);
      wov[33] = *(const v4u*)(wdb + 512);
    }
  }
  STAMP(4);
  {
    f32x4 s = {0.f, 0.f, 0.f, 0.f};
#pragma unroll
    for (int ks = 0; ks < 16; ++ks)
      s = mfma(LD128(L_KV + (16 * wave + r) * PITCH + ks * 64 + g * 16), LD128(L_Q + r * PITCH + ks * 64 + g * 16), s);
    float mx = fmaxf(fmaxf(s.x, s.y), fmaxf(s.z, s.w));
    mx = fmaxf(mx, __shfl_xor(mx, 16));
    mx = fmaxf(mx, __shfl_xor(mx, 32));
    if (g == 0) *(LAS float*)(L + L_SM + (r * 8 + wave) * 4) = mx;
    BAR();
    const f32x4 m0 = LDF4(L_SM + r * 32), m1 = LDF4(L_SM + r * 32 + 16);
    float M = fmaxf(fmaxf(fmaxf(m0.x, m0.y), fmaxf(m0.z, m0.w)), fmaxf(fmaxf(m1.x, m1.y), fmaxf(m1.z, m1.w)));
    {
      unsigned u = __float_as_uint(M);
      u = ((int)u >= 0) ? ((u + 0xffu) & 0xffffff00u) : (u & 0xffffff00u);
      M = __uint_as_float(u);
    }
    const float p0 = __builtin_amdgcn_exp2f(s.x - M), p1 = __builtin_amdgcn_exp2f(s.y - M), p2 = __builtin_amdgcn_exp2f(s.z - M), p3 = __builtin_amdgcn_exp2f(s.w - M);
    float ls = (p0 + p1) + (p2 + p3);
    ls += __shfl_xor(ls, 16);
    ls += __shfl_xor(ls, 32);
    if (g == 0) *(LAS float*)(L + L_SM + 512 + (r * 8 + wave) * 4) = ls;
    *(LAS v2u*)(L + L_P + (wave >> 1) * 1024 + lane * 16 + (wave & 1) * 8) = v2u{pk_bf16(p0, p1), pk_bf16(p2, p3)};
    BAR();
    STAMP(5);
    v4u pf[4];
#pragma unroll
    for (int i = 0; i < 4; ++i) pf[i] = LD128(L_P + i * 1024 + lane * 16);
    const int tq = r >> 2, tp = r & 3;
    const int hh = 16 * hg + r;
#pragma unroll
    for (int ct = 0; ct < 4; ++ct) {
      f32x4 o = {0.f, 0.f, 0.f, 0.f};
#pragma unroll
      for (int i = 0; i < 4; ++i) {
        const int a0 = L_KV + (32 * i + 4 * g + tq) * PITCH + (64 * wave + 16 * ct + 4 * tp) * 2;
        const v2u t0 = __builtin_bit_cast(v2u, __builtin_amdgcn_ds_read_tr16_b64_v4i16((LAS s16x4*)(L + a0)));
        const v2u t1 = __builtin_bit_cast(v2u, __builtin_amdgcn_ds_read_tr16_b64_v4i16((LAS s16x4*)(L + a0 + 16 * PITCH)));
        o = mfma(v4u{t0.x, t0.y, t1.x, t1.y}, pf[i], o);
      }
      __builtin_amdgcn_raw_buffer_store_b128(tag4(o, tag), rs, W_H3 + ((((hh * 8 + wave) * 2 + ab) * 64 + as) * 64 + 16 * ct + 4 * g) * 4, 0, 16);
    }
    if (wave == 0 && g == 0) {
      const f32x4 l0 = LDF4(L_SM + 512 + r * 32), l1 = LDF4(L_SM + 512 + r * 32 + 16);
      const float l = ((l0.x + l0.y) + (l0.z + l0.w)) + ((l1.x + l1.y) + (l1.z + l1.w));
      __builtin_amdgcn_raw_buffer_store_b64(v2u{__float_as_uint(M) | tag, tagw(l, tag)}, rs, W_H3S + ((hh * 2 + ab) * 64 + as) * 8, 0, 16);
    }
  }
  STAMP(6);
  if (wave < 4) {
    const int w = wave, b = w >> 1;
    const int c4 = tid & 15, sg = (tid >> 4) & 7;
    v4u v[8]; v2u st; v4u f0 = {0, 0, 0, 0}, f1 = {0, 0, 0, 0};
    const bool ffn2 = (w == 0) && (bid < 224);
    const unsigned obase = W_H3 + (unsigned)(((h * 8 + j) * 2 + b) * 64 + sg) * 256u + c4 * 16u;
    for (int spin = 0; spin < SPIN_CAP; ++spin) {
      asm volatile("" ::: "memory");
      unsigned bad = 0;
      st = __builtin_amdgcn_raw_buffer_load_b64(rs, W_H3S + ((h * 2 + b) * 64 + lane) * 8, 0, 16);
#pragma unroll
      for (int i = 0; i < 8; ++i) v[i] = __builtin_amdgcn_raw_buffer_load_b128(rs, obase + i * 2048, 0, 16);
      if (ffn2) {
        f0 = __builtin_amdgcn_raw_buffer_load_b128(rs, W_HF + bid * 2048 + lane * 16, 0, 16);
        f1 = __builtin_amdgcn_raw_buffer_load_b128(rs, W_HF + bid * 2048 + 1024 + lane * 16, 0, 16);
        bad |= bad4(f0, tag) | bad4(f1, tag);
      }
      bad |= ((st.x ^ tag) | (st.y ^ tag)) & 0xffu;
#pragma unroll
      for (int i = 0; i < 8; ++i) bad |= bad4(v[i], tag);
      if (__all(bad == 0)) break;
      __builtin_amdgcn_s_sleep(2);
    }
    STAMP(7);
    const float ms = untag(st.x), lsv = untag(st.y);
    float M = ms;
#pragma unroll
    for (int o = 1; o < 64; o <<= 1) M = fmaxf(M, __shfl_xor(M, o));
    const float e = __builtin_amdgcn_exp2f(ms - M);
    float Ls = e * lsv;
#pragma unroll
    for (int o = 1; o < 64; o <<= 1) Ls += __shfl_xor(Ls, o);
    const float wl = e / Ls;
    f32x4 o4 = {0.f, 0.f, 0.f, 0.f};
#pragma unroll
    for (int i = 0; i < 8; ++i) {
      const float ws_ = __shfl(wl, 8 * i + sg);
      o4.x += ws_ * untag(v[i].x); o4.y += ws_ * untag(v[i].y); o4.z += ws_ * untag(v[i].z); o4.w += ws_ * untag(v[i].w);
    }
#pragma unroll
    for (int e2 = 0; e2 < 4; ++e2) { float t = o4[e2]; t += __shfl_xor(t, 16); t += __shfl_xor(t, 32); o4[e2] = t; }
    if (g == 0) *(LAS f32x4*)(L + L_OL + w * 256 + c4 * 16) = o4;
    asm volatile("s_waitcnt lgkmcnt(0)" ::: "memory");
    f32x4 cacc = {0.f, 0.f, 0.f, 0.f};
    {
      const int hl = r >> 3;
#pragma unroll
      for (int q4 = 0; q4 < 4; ++q4) {
        const f32x4 ov = LDF4(L_OL + w * 256 + (16 * g + 4 * q4) * 4);
        unsigned h01 = pk_bf16(ov.x, ov.y), h23 = pk_bf16(ov.z, ov.w);
        const unsigned l01 = pk_bf16(ov.x - bf_lo(h01), ov.y - bf_hi(h01)), l23 = pk_bf16(ov.z - bf_lo(h23), ov.w - bf_hi(h23));
        h01 = hl ? l01 : h01; h23 = hl ? l23 : h23;
        cacc = mfma(buildA(h01, 0), wvv[4 * q4 + 0], cacc);
        cacc = mfma(buildA(h01, 1), wvv[4 * q4 + 1], cacc);
        cacc = mfma(buildA(h23, 0), wvv[4 * q4 + 2], cacc);
        cacc = mfma(buildA(h23, 1), wvv[4 * q4 + 3], cacc);
      }
    }
    const float gb = b ? gate1 : gate0;
#pragma unroll
    for (int e2 = 0; e2 < 4; ++e2) { float t = cacc[e2]; t += __shfl_xor(t, 32); cacc[e2] = t * gb; }
    if (g < 2) *(LAS f32x4*)(L + L_CTX + w * 512 + (8 * r + 4 * g) * 4) = cacc;
    if (w == 0) {
      f32x4 ga = {untag(f0.x), untag(f0.y), untag(f0.z), untag(f0.w)}, ua = {untag(f1.x), untag(f1.y), untag(f1.z), untag(f1.w)};
#pragma unroll
      for (int e2 = 0; e2 < 4; ++e2) {
        float t = ga[e2]; t += __shfl_xor(t, 4); t += __shfl_xor(t, 8); t += __shfl_xor(t, 16); t += __shfl_xor(t, 32); ga[e2] = t;
        float u = ua[e2]; u += __shfl_xor(u, 4); u += __shfl_xor(u, 8); u += __shfl_xor(u, 16); u += __shfl_xor(u, 32); ua[e2] = u;
      }
      const float og = 1.f - (((lane >> 1) & 1) ? gate1 : gate0);
      f32x4 hv;
#pragma unroll
      for (int e2 = 0; e2 < 4; ++e2) {
        const float z = ga[e2];
        const float ge = 0.5f * z * (1.f + tanhf(0.7978845608028654f * (z + 0.044715f * z * z * z)));
        hv[e2] = ffn2 ? ge * ua[e2] * og : 0.f;
      }
      if (lane < 4) *(LAS f32x4*)(L + L_HB + lane * 16) = hv;
    }
    STAMP(8);
    BAR();
    if (w == 0) {
      v4u q[4];
      for (int spin = 0; spin < SPIN_CAP; ++spin) {
        asm volatile("" ::: "memory");
        unsigned bad = 0;
#pragma unroll
        for (int i = 0; i < 4; ++i) q[i] = __builtin_amdgcn_raw_buffer_load_b128(rs, W_H4 + bid * 4096 + i * 1024 + lane * 16, 0, 16);
#pragma unroll
        for (int i = 0; i < 4; ++i) bad |= bad4(q[i], tag);
        if (__all(bad == 0)) break;
        __builtin_amdgcn_s_sleep(2);
      }
      STAMP(9);
      f32x4 t4;
#pragma unroll
      for (int e2 = 0; e2 < 4; ++e2) {
        float t = (untag(q[0][e2]) + untag(q[1][e2])) + (untag(q[2][e2]) + untag(q[3][e2]));
#pragma unroll
        for (int o = 1; o < 64; o <<= 1) t += __shfl_xor(t, o);
        t4[e2] = t;
      }
      if (lane == 0) {
        *(v2u*)(out + bid * 4) = v2u{pk_bf16(t4.x, t4.y), pk_bf16(t4.z, t4.w)};
        if (bid == 0) __builtin_amdgcn_raw_buffer_store_b32(epoch + 1u, rs, W_EPOCH, 0, 16);
      }
      STAMP(10);
    }
  } else {
    const int w = wave - 4;
    BAR();
    STAMP(8);
    const int b = r >> 3;
    f32x4 acc = {0.f, 0.f, 0.f, 0.f};
#pragma unroll
    for (int q4 = 0; q4 < 8; ++q4) {
      const f32x4 c0 = LDF4(L_CTX + (2 * b) * 512 + (32 * g + 4 * q4) * 4), c1 = LDF4(L_CTX + (2 * b + 1) * 512 + (32 * g + 4 * q4) * 4);
      const unsigned p01 = pk_bf16(c0.x + c1.x, c0.y + c1.y), p23 = pk_bf16(c0.z + c1.z, c0.w + c1.w);
      acc = mfma(buildA(p01, 0), wov[4 * q4 + 0], acc);
      acc = mfma(buildA(p01, 1), wov[4 * q4 + 1], acc);
      acc = mfma(buildA(p23, 0), wov[4 * q4 + 2], acc);
      acc = mfma(buildA(p23, 1), wov[4 * q4 + 3], acc);
    }
    {
      const float h0 = *(const LAS float*)(L + L_HB + (b * 8 + 2 * g) * 4), h1 = *(const LAS float*)(L + L_HB + (b * 8 + 2 * g + 1) * 4);
      const unsigned hp = pk_bf16(h0, h1);
      const unsigned lp = pk_bf16(h0 - bf_lo(hp), h1 - bf_hi(hp));
      acc = mfma(buildA(hp, 0), wov[32], acc);
      acc = mfma(buildA(hp, 1), wov[33], acc);
      acc = mfma(buildA(lp, 0), wov[32], acc);
      acc = mfma(buildA(lp, 1), wov[33], acc);
    }
    const unsigned cons = (g >> 1) * 128 + 32 * w + 2 * r + (g & 1);
    __builtin_amdgcn_raw_buffer_store_b128(tag4(acc, tag), rs, W_H4 + cons * 4096 + bid * 16, 0, 16);
    STAMP(9);
  }
}
}

extern "C" void kernel_launch(void* const* d_in, const int* in_sizes, int n_in, void* d_out, int out_size, void* d_ws, size_t ws_size,
                              hipStream_t stream) {
  (void)in_sizes; (void)n_in; (void)out_size; (void)ws_size;
  (void)hipFuncSetAttribute((const void*)mk, hipFuncAttributeMaxDynamicSharedMemorySize, L_END);
  mk<<<256, 512, L_END, stream>>>((const u16*)d_in[0], (const u16*)d_in[1], (const u16*)d_in[2], (const u16*)d_in[3], (const u16*)d_in[4],
                                  (const u16*)d_in[5], (const u16*)d_in[7], (const u16*)d_in[8], (const u16*)d_in[9], (const u16*)d_in[10],
                                  (u16*)d_out, (unsigned char*)d_ws);
}
```

```cpp
#include <hip/hip_runtime.h>
#include <hip/hip_bf16.h>
#include <stdint.h>

typedef unsigned short u16;
typedef unsigned v4u __attribute__((ext_vector_type(4)));
typedef unsigned v2u __attribute__((ext_vector_type(2)));
typedef float f32x4 __attribute__((ext_vector_type(4)));
typedef short s16x4 __attribute__((ext_vector_type(4)));
typedef __bf16 bf16x8 __attribute__((ext_vector_type(8)));
#define LAS __attribute__((address_space(3)))
#define GAS __attribute__((address_space(1)))

namespace {
constexpr int PITCH = 1056;
constexpr int L_KV = 0;
constexpr int L_Q = 128 * PITCH;
constexpr int L_P = L_Q + 16 * PITCH;
constexpr int L_SM = L_P + 4608;
constexpr int L_OL = L_Q;
constexpr int L_CTX = L_OL + 1024;
constexpr int L_PK = L_CTX + 4096;
constexpr int L_CNT = L_SM + 512;
constexpr int L_END = L_CNT + 64;
static_assert(L_END <= 160 * 1024, "LDS");
enum { C_S1 = 0, C_S2, C_KV, C_S3, C_S4, C_S5, C_CTX, C_ST, C_FIN, C_KVH, C_PUB, C_ISS };
constexpr unsigned OOB = 0xfffffff0u;

constexpr unsigned W_EPOCH = 0;
constexpr unsigned W_H2 = 4096;
constexpr unsigned W_HF = W_H2 + 8 * 2 * 32 * 512 * 4;
constexpr unsigned W_H4 = W_HF + 224 * 2048;
constexpr unsigned W_H3 = W_H4 + 256 * 256 * 16;
constexpr unsigned W_DBG = W_H3 + 32 * 8 * 2 * 64 * 64 * 4;
constexpr unsigned W_END = W_DBG + 256 * 2 * 16 * 8;
#define H3_SLEEP 8
#define H2_DELAY 20
#define H3_GATE 1
#define KVH_N 20
#define WO_A 16
constexpr int SPIN_CAP = 1 << 15;
constexpr int LSPIN_CAP = 1 << 22;

__device__ __forceinline__ f32x4 mfma(v4u a, v4u b, f32x4 c) {
  return __builtin_amdgcn_mfma_f32_16x16x32_bf16(__builtin_bit_cast(bf16x8, a), __builtin_bit_cast(bf16x8, b), c, 0, 0, 0);
}
__device__ __forceinline__ float bf_lo(unsigned w) { return __uint_as_float(w << 16); }
__device__ __forceinline__ float bf_hi(unsigned w) { return __uint_as_float(w & 0xffff0000u); }
__device__ __forceinline__ unsigned pk_bf16(float a, float b) {
  typedef __bf16 bf16x2 __attribute__((ext_vector_type(2)));
  typedef float f32x2 __attribute__((ext_vector_type(2)));
  f32x2 v = {a, b};
  bf16x2 r = __builtin_convertvector(v, bf16x2);
  return __builtin_bit_cast(unsigned, r);
}
constexpr unsigned TMASK = 0xfffu;
__device__ __forceinline__ unsigned tagw(float v, unsigned tag) { return ((__float_as_uint(v) + 0x800u) & ~TMASK) | tag; }
__device__ __forceinline__ float untag(unsigned w) { return __uint_as_float(w & ~TMASK); }
__device__ __forceinline__ v4u tag4(f32x4 v, unsigned tag) { return v4u{tagw(v.x, tag), tagw(v.y, tag), tagw(v.z, tag), tagw(v.w, tag)}; }
__device__ __forceinline__ unsigned bad4(v4u w, unsigned tag) { return ((w.x ^ tag) | (w.y ^ tag) | (w.z ^ tag) | (w.w ^ tag)) & TMASK; }
template <int N> __device__ __forceinline__ float dpp_ror(float v) {
  return __int_as_float(__builtin_amdgcn_update_dpp(0, __float_as_int(v), 0x120 + N, 0xf, 0xf, false));
}
__device__ __forceinline__ float x16_add(float v) { auto p = __builtin_amdgcn_permlane16_swap(__float_as_uint(v), __float_as_uint(v), false, false); return __uint_as_float(p[0]) + __uint_as_float(p[1]); }
__device__ __forceinline__ float x32_add(float v) { auto p = __builtin_amdgcn_permlane32_swap(__float_as_uint(v), __float_as_uint(v), false, false); return __uint_as_float(p[0]) + __uint_as_float(p[1]); }
__device__ __forceinline__ float x16_max(float v) { auto p = __builtin_amdgcn_permlane16_swap(__float_as_uint(v), __float_as_uint(v), false, false); return fmaxf(__uint_as_float(p[0]), __uint_as_float(p[1])); }
__device__ __forceinline__ float x32_max(float v) { auto p = __builtin_amdgcn_permlane32_swap(__float_as_uint(v), __float_as_uint(v), false, false); return fmaxf(__uint_as_float(p[0]), __uint_as_float(p[1])); }
__device__ __forceinline__ float wave_sum(float v) { v += dpp_ror<8>(v); v += dpp_ror<4>(v); v += dpp_ror<2>(v); v += dpp_ror<1>(v); v = x16_add(v); return x32_add(v); }
__device__ __forceinline__ float wave_max(float v) { v = fmaxf(v, dpp_ror<8>(v)); v = fmaxf(v, dpp_ror<4>(v)); v = fmaxf(v, dpp_ror<2>(v)); v = fmaxf(v, dpp_ror<1>(v)); v = x16_max(v); return x32_max(v); }
__device__ __forceinline__ v4u ldnt(const u16* p) { return __builtin_nontemporal_load((const v4u*)p); }

#define LD128(off) (*(const LAS v4u*)(L + (off)))
#define LDF4(off) (*(const LAS f32x4*)(L + (off)))
#define FENCE() do { __builtin_amdgcn_sched_barrier(0); asm volatile("" ::: "memory"); } while (0)

#define STAMP(i)
#define STAMP_FLUSH()
#define L_ALLOC L_END

__global__ __launch_bounds__(512, 2) void mk(const u16* __restrict__ x, const u16* __restrict__ ckv, const u16* __restrict__ wq,
                                             const u16* __restrict__ wuk, const u16* __restrict__ wuv, const u16* __restrict__ wo,
                                             const u16* __restrict__ wg, const u16* __restrict__ wu, const u16* __restrict__ wd,
                                             const u16* __restrict__ gw, u16* __restrict__ out, unsigned char* ws) {
  extern __shared__ __attribute__((aligned(16))) unsigned char lds[];
  LAS unsigned char* L = (LAS unsigned char*)lds;
  const int tid = threadIdx.x, lane = tid & 63, wave = __builtin_amdgcn_readfirstlane(tid >> 6);
  const int bid = blockIdx.x, xcd = bid & 7, slot = bid >> 3;
  const int h = xcd * 4 + (slot >> 3), j = slot & 7;
  const int ab = xcd >> 2, as = (xcd & 3) * 16 + (slot >> 1), hg = slot & 1;
  const int g = lane >> 4, r = lane & 15, jj = r & 7;
  const auto rs = __builtin_amdgcn_make_buffer_rsrc(ws, 0, W_END, 0x00020000);
  STAMP(0);
  int vzero = 0;
  asm volatile("" : "+v"(vzero));
  unsigned selE[4], selO[4];
#pragma unroll
  for (int k = 0; k < 4; ++k) {
    selE[k] = (k == (jj >> 1)) ? ((jj & 1) ? 0x01000c0cu : 0x0c0c0100u) : 0x0c0c0c0cu;
    selO[k] = (k == (jj >> 1)) ? ((jj & 1) ? 0x03020c0cu : 0x0c0c0302u) : 0x0c0c0c0cu;
  }
  auto buildA = [&](unsigned pw, int odd) -> v4u {
    v4u a;
    if (odd) { a.x = __builtin_amdgcn_perm(pw, pw, selO[0]); a.y = __builtin_amdgcn_perm(pw, pw, selO[1]); a.z = __builtin_amdgcn_perm(pw, pw, selO[2]); a.w = __builtin_amdgcn_perm(pw, pw, selO[3]); }
    else     { a.x = __builtin_amdgcn_perm(pw, pw, selE[0]); a.y = __builtin_amdgcn_perm(pw, pw, selE[1]); a.z = __builtin_amdgcn_perm(pw, pw, selE[2]); a.w = __builtin_amdgcn_perm(pw, pw, selE[3]); }
    return a;
  };
  auto arrive = [&](int c) {
    FENCE();
    if (lane == 0) __hip_atomic_fetch_add((LAS unsigned*)(L + L_CNT + 4 * c), 1u, __ATOMIC_RELAXED, __HIP_MEMORY_SCOPE_WORKGROUP);
    FENCE();
  };
  auto await = [&](int c, unsigned n) {
    FENCE();
    for (int s = 0; s < LSPIN_CAP; ++s) {
      if (*(volatile LAS unsigned*)(L + L_CNT + 4 * c) >= n) break;
    }
    FENCE();
  };

#define GATHER(N, v, base, stride, tagv) GATHERX(N, v, base, stride, tagv, , 0u)
#define GATHERX(N, v, base, stride, tagv, EXTRA_LOAD, EXTRA_BAD)                                                \
  {                                                                                                             \
    unsigned pend_ = (1u << (N)) - 1u;                                                                          \
    for (int spin_ = 0; spin_ < SPIN_CAP; ++spin_) {                                                            \
      asm volatile("" ::: "memory");                                                                            \
      v4u t_[N];                                                                                                \
      EXTRA_LOAD;                                                                                               \
      _Pragma("unroll") for (int i_ = 0; i_ < (N); ++i_)                                                        \
        t_[i_] = __builtin_amdgcn_raw_buffer_load_b128(rs, ((pend_ >> i_) & 1u) ? (base) + i_ * (stride) : OOB, 0, 16); \
      _Pragma("unroll") for (int i_ = 0; i_ < (N); ++i_) {                                                      \
        const bool p_ = (pend_ >> i_) & 1u;                                                                     \
        v[i_].x = p_ ? t_[i_].x : v[i_].x; v[i_].y = p_ ? t_[i_].y : v[i_].y;                                   \
        v[i_].z = p_ ? t_[i_].z : v[i_].z; v[i_].w = p_ ? t_[i_].w : v[i_].w;                                   \
        if (bad4(v[i_], tagv) == 0) pend_ &= ~(1u << i_);                                                       \
      }                                                                                                         \
      if (__all((pend_ | (EXTRA_BAD)) == 0)) break;                                                             \
      __builtin_amdgcn_s_sleep(1);                                                                              \
    }                                                                                                           \
  }

  if (wave < 4) {
    const int w = wave;
    unsigned tag, epoch;
    {
      const u16* wkb = wuk + ((size_t)(64 * j + 16 * w + r) * 32 + h) * 128 + 8 * g;
      v4u wk[4];
#pragma unroll
      for (int ks = 0; ks < 4; ++ks) wk[ks] = ldnt(wkb + 32 * ks);
      const u16* xp = x + (r >> 3) * 512 + 128 * w + 32 * g;
      v2u xv[8];
#pragma unroll
      for (int k = 0; k < 8; ++k) xv[k] = *(const v2u*)(xp + ((4 * k + 4 * j) & 31));
      const u16* wqb = wq + (size_t)(128 * w + 32 * g) * 4096 + h * 128 + 8 * r;
      v4u wv[32];
#pragma unroll
      for (int i = 0; i < 32; ++i) wv[i] = ldnt(wqb + (size_t)((i + 4 * j) & 31) * 4096);
      epoch = __builtin_amdgcn_raw_buffer_load_b32(rs, vzero + W_EPOCH, 0, 16);
      FENCE();
      __builtin_amdgcn_s_barrier();
      FENCE();
      f32x4 acc = {0.f, 0.f, 0.f, 0.f};
#pragma unroll
      for (int i = 0; i < 32; ++i) acc = mfma(buildA(xv[i >> 2][(i >> 1) & 1], i & 1), wv[i], acc);
      STAMP(1);
      {
        const unsigned h01 = pk_bf16(acc.x, acc.y), h23 = pk_bf16(acc.z, acc.w);
        const unsigned l01 = pk_bf16(acc.x - bf_lo(h01), acc.y - bf_hi(h01)), l23 = pk_bf16(acc.z - bf_lo(h23), acc.w - bf_hi(h23));
        const int b = g >> 1, d0 = 8 * r + 4 * (g & 1);
        *(LAS v2u*)(L + L_P + (b + 4 * w) * 288 + d0 * 2) = v2u{h01, h23};
        *(LAS v2u*)(L + L_P + (b + 2 + 4 * w) * 288 + d0 * 2) = v2u{l01, l23};
      }
      arrive(C_S1);
      await(C_S1, 4);
      f32x4 qa = {0.f, 0.f, 0.f, 0.f};
#pragma unroll
      for (int ks = 0; ks < 4; ++ks) qa = mfma(wk[ks], LD128(L_P + r * 288 + ks * 64 + g * 16), qa);
#pragma unroll
      for (int e = 0; e < 4; ++e) { float v = qa[e]; v += dpp_ror<8>(v); v += dpp_ror<4>(v); v += dpp_ror<2>(v); qa[e] = v * (0.08838834764831845f * 1.4426950408889634f); }
      tag = (epoch + 1u) & TMASK;
      __builtin_amdgcn_raw_buffer_store_b128(tag4(qa, tag), rs, W_H2 + ((r * 32 + h) * 512 + 64 * j + 16 * w + 4 * g) * 4, 0, 16);
    }
    STAMP(2);
    arrive(C_PUB);
    await(C_ISS, 4);
    STAMP(3);
    v4u kf[32];
    {
      v4u v[8] = {};
      const unsigned rep = (xcd & 3) * 2 + ((slot >> 1) & 1);
      const unsigned base = W_H2 + (unsigned)((rep * 2 + ab) * 32 + hg * 16) * 2048u + (unsigned)tid * 16u;
      GATHER(8, v, base, 4096u, tag);
#pragma unroll
      for (int i = 0; i < 8; ++i) {
        const int head = 2 * i + (tid >> 7), c = 4 * (tid & 127);
        *(LAS v2u*)(L + L_Q + head * PITCH + c * 2) = v2u{pk_bf16(untag(v[i].x), untag(v[i].y)), pk_bf16(untag(v[i].z), untag(v[i].w))};
      }
    }
    arrive(C_S2);
    await(C_KV, 4);
#pragma unroll
    for (int ks = 0; ks < 16; ++ks) {
      kf[2 * ks] = LD128(L_KV + (32 * w + r) * PITCH + ks * 64 + g * 16);
      kf[2 * ks + 1] = LD128(L_KV + (32 * w + 16 + r) * PITCH + ks * 64 + g * 16);
    }
    v4u wvv[4];
    {
      const u16* wvb = wuv + ((size_t)(64 * j + 16 * w + 4 * g) * 32 + h) * 128 + 8 * r;
#pragma unroll
      for (int i = 0; i < 4; ++i) wvv[i] = ldnt(wvb + (size_t)i * 4096);
    }
    await(C_S2, 4);
    STAMP(4);
    {
      f32x4 s0 = {0.f, 0.f, 0.f, 0.f}, s1 = {0.f, 0.f, 0.f, 0.f};
      const int tq = r >> 2, tp = r & 3;
      v4u vf[32];
      {
        v4u qf[16];
#pragma unroll
        for (int ks = 0; ks < 16; ++ks) qf[ks] = LD128(L_Q + r * PITCH + ks * 64 + g * 16);
        FENCE();
#pragma unroll
        for (int ks = 0; ks < 16; ++ks) {
          s0 = mfma(kf[2 * ks], qf[ks], s0);
          s1 = mfma(kf[2 * ks + 1], qf[ks], s1);
#pragma unroll
          for (int e = 0; e < 2; ++e) {
            const int ct = ks >> 1, i = 2 * (ks & 1) + e;
            const int a0 = L_KV + (32 * i + 4 * g + tq) * PITCH + (128 * w + 16 * ct + 4 * tp) * 2;
            const v2u t0 = __builtin_bit_cast(v2u, __builtin_amdgcn_ds_read_tr16_b64_v4i16((LAS s16x4*)(L + a0)));
            const v2u t1 = __builtin_bit_cast(v2u, __builtin_amdgcn_ds_read_tr16_b64_v4i16((LAS s16x4*)(L + a0 + 16 * PITCH)));
            vf[4 * ct + i] = v4u{t0.x, t0.y, t1.x, t1.y};
          }
          __builtin_amdgcn_sched_barrier(0);
        }
      }
      FENCE();
      float mx = fmaxf(fmaxf(fmaxf(s0.x, s0.y), fmaxf(s0.z, s0.w)), fmaxf(fmaxf(s1.x, s1.y), fmaxf(s1.z, s1.w)));
      mx = x32_max(x16_max(mx));
      if (g == 0) *(LAS float*)(L + L_SM + (r * 4 + w) * 4) = mx;
      arrive(C_S3);
      await(C_S3, 4);
      const f32x4 m0 = LDF4(L_SM + r * 16);
      float M = fmaxf(fmaxf(m0.x, m0.y), fmaxf(m0.z, m0.w));
      f32x4 p0, p1;
#pragma unroll
      for (int e = 0; e < 4; ++e) { p0[e] = __builtin_amdgcn_exp2f(s0[e] - M); p1[e] = __builtin_amdgcn_exp2f(s1[e] - M); }
      *(LAS v4u*)(L + L_P + w * 1024 + lane * 16) = v4u{pk_bf16(p0.x, p0.y), pk_bf16(p0.z, p0.w), pk_bf16(p1.x, p1.y), pk_bf16(p1.z, p1.w)};
      float ls = ((p0.x + p0.y) + (p0.z + p0.w)) + ((p1.x + p1.y) + (p1.z + p1.w));
      ls = x32_add(x16_add(ls));
      if (g == 0) *(LAS float*)(L + L_SM + 256 + (r * 4 + w) * 4) = ls;
      arrive(C_S4);
      await(C_S4, 4);
      STAMP(5);
      v4u pf[4];
#pragma unroll
      for (int i = 0; i < 4; ++i) pf[i] = LD128(L_P + i * 1024 + lane * 16);
      unsigned lsb;
      float osc;
      {
        const f32x4 l0 = LDF4(L_SM + 256 + r * 16);
        const float lse = M + __builtin_amdgcn_logf((l0.x + l0.y) + (l0.z + l0.w));
        lsb = (__float_as_uint(lse) + 0x80u) & 0xffffff00u;
        osc = __builtin_amdgcn_exp2f(M - __uint_as_float(lsb));
      }
      const unsigned f1 = lsb >> 20, f3 = (lsb >> 8) & TMASK;
      const int stg = L_KV + w * (32 * 272);
#pragma unroll
      for (int ct = 0; ct < 8; ++ct) {
        f32x4 o = {0.f, 0.f, 0.f, 0.f};
#pragma unroll
        for (int i = 0; i < 4; ++i) o = mfma(vf[4 * ct + i], pf[i], o);
        o *= osc;
        *(LAS v4u*)(L + stg + ((ct >> 2) * 16 + r) * 272 + ((ct & 3) * 4 + g) * 16) = v4u{tagw(o.x, tag), tagw(o.y, f1), tagw(o.z, tag), tagw(o.w, f3)};
      }
      FENCE();
#pragma unroll
      for (int k = 0; k < 8; ++k) {
        const v4u d = LD128(stg + (4 * k + g) * 272 + r * 16);
        __builtin_amdgcn_raw_buffer_store_b128(d, rs, W_H3 + (((ab * 64 + as) * 2 + hg) * 32 + 8 * w + k) * 1024 + lane * 16, 0, 16);
      }
    }
    arrive(C_ST);
    STAMP(6);
    await(C_KVH, 4);
    {
      const int b = w >> 1;
      const int c4 = tid & 15, sg = (tid >> 4) & 7;
      v4u v[8] = {};
      const unsigned obase = W_H3 + (unsigned)(((b * 64 + sg) * 2 + (h >> 4)) * 32 + 4 * j) * 1024u + (h & 15) * 256u + c4 * 16u;
      {
        unsigned pend = 0xffu;
        for (int spin = 0; spin < SPIN_CAP; ++spin) {
          asm volatile("" ::: "memory");
          v4u t[8];
#pragma unroll
          for (int i = 0; i < 8; ++i) t[i] = __builtin_amdgcn_raw_buffer_load_b128(rs, ((pend >> i) & 1u) ? obase + i * (8u * 65536u) : OOB, 0, 16);
          unsigned badm = 0;
#pragma unroll
          for (int i = 0; i < 8; ++i) badm |= ((((t[i].x ^ tag) | (t[i].z ^ tag)) & TMASK) ? 1u : 0u) << i;
          badm |= (unsigned)__builtin_amdgcn_update_dpp(0, (int)badm, 0x128, 0xf, 0xf, false);
          badm |= (unsigned)__builtin_amdgcn_update_dpp(0, (int)badm, 0x124, 0xf, 0xf, false);
          badm |= (unsigned)__builtin_amdgcn_update_dpp(0, (int)badm, 0x122, 0xf, 0xf, false);
          badm |= (unsigned)__builtin_amdgcn_update_dpp(0, (int)badm, 0x121, 0xf, 0xf, false);
#pragma unroll
          for (int i = 0; i < 8; ++i) {
            const bool p = (pend >> i) & 1u;
            v[i].x = p ? t[i].x : v[i].x; v[i].y = p ? t[i].y : v[i].y; v[i].z = p ? t[i].z : v[i].z; v[i].w = p ? t[i].w : v[i].w;
          }
          pend &= badm;
          if (__all(pend == 0)) break;
          __builtin_amdgcn_s_sleep(H3_SLEEP);
        }
      }
      STAMP(7);
      float ls[8];
      float Mw = -3.0e38f;
#pragma unroll
      for (int i = 0; i < 8; ++i) { ls[i] = __uint_as_float(((v[i].y & TMASK) << 20) | ((v[i].w & TMASK) << 8)); Mw = fmaxf(Mw, ls[i]); }
      Mw = x32_max(x16_max(Mw));
      f32x4 o4 = {0.f, 0.f, 0.f, 0.f};
      float Lw = 0.f;
#pragma unroll
      for (int i = 0; i < 8; ++i) {
        const float ws_ = __builtin_amdgcn_exp2f(ls[i] - Mw);
        Lw += ws_;
        o4.x += ws_ * untag(v[i].x); o4.y += ws_ * untag(v[i].y); o4.z += ws_ * untag(v[i].z); o4.w += ws_ * untag(v[i].w);
      }
#pragma unroll
      for (int e2 = 0; e2 < 4; ++e2) o4[e2] = x32_add(x16_add(o4[e2]));
      Lw = x32_add(x16_add(Lw));
      if (g == 0) *(LAS f32x4*)(L + L_OL + w * 256 + c4 * 16) = o4;
      if (lane == 0) *(LAS v2u*)(L + L_SM + w * 8) = v2u{__float_as_uint(Mw), __float_as_uint(Lw)};
    }
    arrive(C_S5);
    await(C_S5, 4);
    {
      const int b = r >> 3;
      const f32x4 oa = LDF4(L_OL + (2 * b) * 256 + (16 * w + 4 * g) * 4), ob = LDF4(L_OL + (2 * b + 1) * 256 + (16 * w + 4 * g) * 4);
      const f32x4 ml = LDF4(L_SM + b * 16);
      const float Mb = fmaxf(ml.x, ml.z);
      const float ea = __builtin_amdgcn_exp2f(ml.x - Mb), eb = __builtin_amdgcn_exp2f(ml.z - Mb);
      const float inv = 1.f / (ml.y * ea + ml.w * eb);
      const f32x4 ov = oa * (ea * inv) + ob * (eb * inv);
      const unsigned h01 = pk_bf16(ov.x, ov.y), h23 = pk_bf16(ov.z, ov.w);
      const unsigned l01 = pk_bf16(ov.x - bf_lo(h01), ov.y - bf_hi(h01)), l23 = pk_bf16(ov.z - bf_lo(h23), ov.w - bf_hi(h23));
      f32x4 c0 = {0.f, 0.f, 0.f, 0.f}, c1 = {0.f, 0.f, 0.f, 0.f};
      c0 = mfma(buildA(h01, 0), wvv[0], c0); c1 = mfma(buildA(l01, 0), wvv[0], c1);
      c0 = mfma(buildA(h01, 1), wvv[1], c0); c1 = mfma(buildA(l01, 1), wvv[1], c1);
      c0 = mfma(buildA(h23, 0), wvv[2], c0); c1 = mfma(buildA(l23, 0), wvv[2], c1);
      c0 = mfma(buildA(h23, 1), wvv[3], c0); c1 = mfma(buildA(l23, 1), wvv[3], c1);
      *(LAS f32x4*)(L + L_CTX + w * 1024 + (g >> 1) * 512 + (8 * r + 4 * (g & 1)) * 4) = c0 + c1;
    }
    arrive(C_CTX);
    STAMP(8);
    if ((bid & 3) == 0) {
      const int oc = bid >> 2;
      v4u q[4] = {};
      const unsigned qbase = W_H4 + (unsigned)(64 * w + (lane >> 2)) * 4096u + oc * 64u + (lane & 3) * 16u;
      GATHER(4, q, qbase, 16u * 4096u, tag);
      STAMP(9);
      f32x4 t4;
#pragma unroll
      for (int e2 = 0; e2 < 4; ++e2) {
        float t = (untag(q[0][e2]) + untag(q[1][e2])) + (untag(q[2][e2]) + untag(q[3][e2]));
        t += dpp_ror<8>(t); t += dpp_ror<4>(t);
        t4[e2] = x32_add(x16_add(t));
      }
      if (lane < 4) *(LAS f32x4*)(L + L_SM + 256 + w * 64 + lane * 16) = t4;
      arrive(C_FIN);
      if (w == 0) {
        await(C_FIN, 4);
        if (lane < 4) {
          const f32x4 o = (LDF4(L_SM + 256 + lane * 16) + LDF4(L_SM + 320 + lane * 16)) + (LDF4(L_SM + 384 + lane * 16) + LDF4(L_SM + 448 + lane * 16));
          *(v2u*)(out + oc * 16 + lane * 4) = v2u{pk_bf16(o.x, o.y), pk_bf16(o.z, o.w)};
          if (bid == 0 && lane == 0) __builtin_amdgcn_raw_buffer_store_b32(epoch + 1u, rs, W_EPOCH, 0, 16);
        }
      }
    }
    STAMP(10);
    STAMP_FLUSH();
  } else {
    const int w = wave - 4;
    if (w == 0 && lane < 16) *(LAS unsigned*)(L + L_CNT + 4 * lane) = 0u;
    asm volatile("s_waitcnt lgkmcnt(0)" ::: "memory");
    FENCE();
    __builtin_amdgcn_s_barrier();
    FENCE();
    const v4u gx0 = *(const v4u*)(x + lane * 8), gx1 = *(const v4u*)(x + 512 + lane * 8), gwv = *(const v4u*)(gw + lane * 8);
    const unsigned epoch = __builtin_amdgcn_raw_buffer_load_b32(rs, vzero + W_EPOCH, 0, 16);
    await(C_PUB, 4);
    const u16* src = ckv + ((size_t)ab * 8192 + as * 128 + w * 32) * 512 + lane * 8;
#pragma unroll
    for (int i = 0; i < 32; ++i)
      __builtin_amdgcn_global_load_lds((const void*)(src + i * 512), (LAS void*)(L + L_KV + (w * 32 + i) * PITCH), 16, 0, 2);
    FENCE();
    if (lane == 0) asm volatile("ds_add_u32 %0, %1" ::"v"((unsigned)(L_CNT + 4 * C_ISS)), "v"(1u) : "memory");
    FENCE();
    __builtin_amdgcn_s_waitcnt(0x0f70);
    arrive(C_KV);
    STAMP(1);
    v4u fv[8]; v4u fx;
    const bool ffn1 = (w < 2) && (bid < 224);
    const int cb = bid % 14, rb = bid / 14;
    await(C_S2, 4);
    if (ffn1) {
      const u16* fb = (w == 0 ? wg : wu) + (size_t)(32 * rb + 8 * g) * 1792 + 128 * cb + 8 * r;
#pragma unroll
      for (int i = 0; i < 8; ++i) fv[i] = ldnt(fb + (size_t)i * 1792);
      fx = *(const v4u*)(x + (r >> 3) * 512 + 32 * rb + 8 * g);
    }
    v4u wov[34];
    const u16* wob = wo + ((size_t)h * 128 + 32 * g) * 512 + 128 * w + 8 * r;
#pragma unroll
    for (int i = 0; i < WO_A; ++i) wov[i] = ldnt(wob + ((i + 4 * j) & 31) * 512);
    FENCE();
    const unsigned tag = (epoch + 1u) & TMASK;
    if (ffn1) {
      f32x4 acc = {0.f, 0.f, 0.f, 0.f};
#pragma unroll
      for (int i = 0; i < 8; ++i) acc = mfma(buildA(fx[i >> 1], i & 1), fv[i], acc);
      const unsigned u = 16 * cb + r;
      __builtin_amdgcn_raw_buffer_store_b128(tag4(acc, tag), rs, W_HF + u * 2048 + ((w * 16 + rb) * 4 + g) * 16, 0, 16);
    }
    float gate0, gate1;
    {
      float s0 = 0.f, s1 = 0.f;
#pragma unroll
      for (int k = 0; k < 4; ++k) {
        s0 += bf_lo(gx0[k]) * bf_lo(gwv[k]) + bf_hi(gx0[k]) * bf_hi(gwv[k]);
        s1 += bf_lo(gx1[k]) * bf_lo(gwv[k]) + bf_hi(gx1[k]) * bf_hi(gwv[k]);
      }
      s0 = wave_sum(s0); s1 = wave_sum(s1);
      gate0 = 1.f / (1.f + __expf(-s0));
      gate1 = 1.f / (1.f + __expf(-s1));
    }
    STAMP(2);
    {
      STAMP(4);
      await(C_ST, 4);
      STAMP(5);
#pragma unroll
      for (int i = WO_A; i < 32; ++i) wov[i] = ldnt(wob + ((i + 4 * j) & 31) * 512);
      const int u = bid < 224 ? bid : 0;
      const u16* wdb = wd + (size_t)(8 * u + 2 * g) * 512 + 128 * w + 8 * r;
      wov[32] = ldnt(wdb);
      wov[33] = ldnt(wdb + 512);
    }
    arrive(C_KVH);
    STAMP(6);
    unsigned hp = 0u, lp = 0u;
    if (bid < 224) {
      v4u ff[2] = {};
      const unsigned fbase = W_HF + bid * 2048 + lane * 16;
      GATHER(2, ff, fbase, 1024u, tag);
      const v4u f0 = ff[0], f1 = ff[1];
      f32x4 hv;
      const float og = 1.f - (((lane >> 1) & 1) ? gate1 : gate0);
#pragma unroll
      for (int e2 = 0; e2 < 4; ++e2) {
        float t = untag(f0[e2]); t += dpp_ror<8>(t); t += dpp_ror<4>(t); t = x32_add(x16_add(t));
        float u = untag(f1[e2]); u += dpp_ror<8>(u); u += dpp_ror<4>(u); u = x32_add(x16_add(u));
        const float a2 = 2.f * 0.7978845608028654f * (t + 0.044715f * t * t * t);
        hv[e2] = t / (1.f + __expf(-a2)) * u * og;
      }
      const int srcl = (r >> 3) * 2 + (g >> 1);
      const float a0 = __shfl(hv.x, srcl), a1 = __shfl(hv.y, srcl), a2_ = __shfl(hv.z, srcl), a3 = __shfl(hv.w, srcl);
      const float h0 = (g & 1) ? a2_ : a0, h1 = (g & 1) ? a3 : a1;
      hp = pk_bf16(h0, h1);
      lp = pk_bf16(h0 - bf_lo(hp), h1 - bf_hi(hp));
    }
    f32x4 acc = {0.f, 0.f, 0.f, 0.f};
    acc = mfma(buildA(hp, 0), wov[32], acc);
    acc = mfma(buildA(hp, 1), wov[33], acc);
    acc = mfma(buildA(lp, 0), wov[32], acc);
    acc = mfma(buildA(lp, 1), wov[33], acc);
    STAMP(7);
    await(C_CTX, 4);
    STAMP(8);
    {
      const int b = r >> 3;
      const float gb = b ? gate1 : gate0;
      const int off = L_CTX + b * 512 + (32 * g + 4 * jj) * 4;
      const f32x4 cs = ((LDF4(off) + LDF4(off + 1024)) + (LDF4(off + 2048) + LDF4(off + 3072))) * gb;
      *(LAS v2u*)(L + L_PK + w * 512 + b * 256 + (16 * g + 2 * jj) * 4) = v2u{pk_bf16(cs.x, cs.y), pk_bf16(cs.z, cs.w)};
      FENCE();
      v2u pk[8];
#pragma unroll
      for (int k = 0; k < 8; ++k) pk[k] = *(const LAS v2u*)(L + L_PK + w * 512 + b * 256 + (16 * g + ((2 * k + 2 * j) & 15)) * 4);
#pragma unroll
      for (int i = 0; i < 32; ++i) acc = mfma(buildA(pk[i >> 2][(i >> 1) & 1], i & 1), wov[i], acc);
    }
    const unsigned cons = (g >> 1) * 128 + 32 * w + 2 * r + (g & 1);
    __builtin_amdgcn_raw_buffer_store_b128(tag4(acc, tag), rs, W_H4 + bid * 4096 + cons * 16, 0, 16);
    STAMP(9);
    STAMP_FLUSH();
  }
}
}

extern "C" void kernel_launch(void* const* d_in, const int* in_sizes, int n_in, void* d_out, int out_size, void* d_ws, size_t ws_size,
                              hipStream_t stream) {
  (void)in_sizes; (void)n_in; (void)out_size; (void)ws_size;
  (void)hipFuncSetAttribute((const void*)mk, hipFuncAttributeMaxDynamicSharedMemorySize, L_ALLOC);
  mk<<<256, 512, L_ALLOC, stream>>>((const u16*)d_in[0], (const u16*)d_in[1], (const u16*)d_in[2], (const u16*)d_in[3], (const u16*)d_in[4],
                                  (const u16*)d_in[5], (const u16*)d_in[7], (const u16*)d_in[8], (const u16*)d_in[9], (const u16*)d_in[10],
                                  (u16*)d_out, (unsigned char*)d_ws);
}
```

```cpp
#include <hip/hip_runtime.h>
#include <hip/hip_bf16.h>
#include <stdint.h>

typedef unsigned short u16;
typedef unsigned v4u __attribute__((ext_vector_type(4)));
typedef unsigned v2u __attribute__((ext_vector_type(2)));
typedef float f32x4 __attribute__((ext_vector_type(4)));
typedef short s16x4 __attribute__((ext_vector_type(4)));
typedef __bf16 bf16x8 __attribute__((ext_vector_type(8)));
#define LAS __attribute__((address_space(3)))
#define GAS __attribute__((address_space(1)))

namespace {
constexpr int PITCH = 1056;
constexpr int L_KV = 0;
constexpr int L_Q = 128 * PITCH;
constexpr int L_P = L_Q + 16 * PITCH;
constexpr int L_SM = L_P + 4608;
constexpr int L_OL = L_Q;
constexpr int L_CTX = L_OL + 1024;
constexpr int L_PK = L_CTX + 4096;
constexpr int L_CNT = L_SM + 512;
constexpr int L_END = L_CNT + 64;
static_assert(L_END <= 160 * 1024, "LDS");
enum { C_S1 = 0, C_S2, C_KV, C_S3, C_S4, C_S5, C_CTX, C_ST, C_FIN, C_KVH, C_PUB, C_ISS };
constexpr unsigned OOB = 0xfffffff0u;

constexpr unsigned W_EPOCH = 0;
constexpr unsigned W_H2 = 4096;
constexpr unsigned W_HF = W_H2 + 8 * 2 * 32 * 512 * 4;
constexpr unsigned W_H4 = W_HF + 224 * 2048;
constexpr unsigned W_H3 = W_H4 + 256 * 256 * 16;
constexpr unsigned W_DBG = W_H3 + 32 * 8 * 2 * 64 * 64 * 4;
constexpr unsigned W_END = W_DBG + 256 * 2 * 16 * 8;
#define H3_SLEEP 8
#define H2_DELAY 20
#define H3_GATE 1
#define ISS_AT 20
#define TLBW 0
#define KVH_N 20
#define WO_A 4
constexpr int SPIN_CAP = 1 << 15;
constexpr int LSPIN_CAP = 1 << 22;

__device__ __forceinline__ f32x4 mfma(v4u a, v4u b, f32x4 c) {
  return __builtin_amdgcn_mfma_f32_16x16x32_bf16(__builtin_bit_cast(bf16x8, a), __builtin_bit_cast(bf16x8, b), c, 0, 0, 0);
}
__device__ __forceinline__ float bf_lo(unsigned w) { return __uint_as_float(w << 16); }
__device__ __forceinline__ float bf_hi(unsigned w) { return __uint_as_float(w & 0xffff0000u); }
__device__ __forceinline__ unsigned pk_bf16(float a, float b) {
  typedef __bf16 bf16x2 __attribute__((ext_vector_type(2)));
  typedef float f32x2 __attribute__((ext_vector_type(2)));
  f32x2 v = {a, b};
  bf16x2 r = __builtin_convertvector(v, bf16x2);
  return __builtin_bit_cast(unsigned, r);
}
constexpr unsigned TMASK = 0xfffu;
__device__ __forceinline__ unsigned tagw(float v, unsigned tag) { return ((__float_as_uint(v) + 0x800u) & ~TMASK) | tag; }
__device__ __forceinline__ float untag(unsigned w) { return __uint_as_float(w & ~TMASK); }
__device__ __forceinline__ v4u tag4(f32x4 v, unsigned tag) { return v4u{tagw(v.x, tag), tagw(v.y, tag), tagw(v.z, tag), tagw(v.w, tag)}; }
__device__ __forceinline__ unsigned bad4(v4u w, unsigned tag) { return ((w.x ^ tag) | (w.y ^ tag) | (w.z ^ tag) | (w.w ^ tag)) & TMASK; }
template <int N> __device__ __forceinline__ float dpp_ror(float v) {
  return __int_as_float(__builtin_amdgcn_update_dpp(0, __float_as_int(v), 0x120 + N, 0xf, 0xf, false));
}
__device__ __forceinline__ float x16_add(float v) { auto p = __builtin_amdgcn_permlane16_swap(__float_as_uint(v), __float_as_uint(v), false, false); return __uint_as_float(p[0]) + __uint_as_float(p[1]); }
__device__ __forceinline__ float x32_add(float v) { auto p = __builtin_amdgcn_permlane32_swap(__float_as_uint(v), __float_as_uint(v), false, false); return __uint_as_float(p[0]) + __uint_as_float(p[1]); }
__device__ __forceinline__ float x16_max(float v) { auto p = __builtin_amdgcn_permlane16_swap(__float_as_uint(v), __float_as_uint(v), false, false); return fmaxf(__uint_as_float(p[0]), __uint_as_float(p[1])); }
__device__ __forceinline__ float x32_max(float v) { auto p = __builtin_amdgcn_permlane32_swap(__float_as_uint(v), __float_as_uint(v), false, false); return fmaxf(__uint_as_float(p[0]), __uint_as_float(p[1])); }
__device__ __forceinline__ float wave_sum(float v) { v += dpp_ror<8>(v); v += dpp_ror<4>(v); v += dpp_ror<2>(v); v += dpp_ror<1>(v); v = x16_add(v); return x32_add(v); }
__device__ __forceinline__ float wave_max(float v) { v = fmaxf(v, dpp_ror<8>(v)); v = fmaxf(v, dpp_ror<4>(v)); v = fmaxf(v, dpp_ror<2>(v)); v = fmaxf(v, dpp_ror<1>(v)); v = x16_max(v); return x32_max(v); }
__device__ __forceinline__ v4u ldnt(const u16* p) { return __builtin_nontemporal_load((const v4u*)p); }

#define LD128(off) (*(const LAS v4u*)(L + (off)))
#define LDF4(off) (*(const LAS f32x4*)(L + (off)))
#define FENCE() do { __builtin_amdgcn_sched_barrier(0); asm volatile("" ::: "memory"); } while (0)

#define STAMP(i)
#define STAMP_FLUSH()
#define L_ALLOC L_END

__global__ __launch_bounds__(512, 2) void mk(const u16* __restrict__ x, const u16* __restrict__ ckv, const u16* __restrict__ wq,
                                             const u16* __restrict__ wuk, const u16* __restrict__ wuv, const u16* __restrict__ wo,
                                             const u16* __restrict__ wg, const u16* __restrict__ wu, const u16* __restrict__ wd,
                                             const u16* __restrict__ gw, u16* __restrict__ out, unsigned char* ws) {
  extern __shared__ __attribute__((aligned(16))) unsigned char lds[];
  LAS unsigned char* L = (LAS unsigned char*)lds;
  const int tid = threadIdx.x, lane = tid & 63, wave = __builtin_amdgcn_readfirstlane(tid >> 6);
  const int bid = blockIdx.x, xcd = bid & 7, slot = bid >> 3;
  const int h = xcd * 4 + (slot >> 3), j = slot & 7;
  const int ab = xcd >> 2, as = (xcd & 3) * 16 + (slot >> 1), hg = slot & 1;
  const int g = lane >> 4, r = lane & 15, jj = r & 7;
  const auto rs = __builtin_amdgcn_make_buffer_rsrc(ws, 0, W_END, 0x00020000);
  STAMP(0);
  int vzero = 0;
  asm volatile("" : "+v"(vzero));
  unsigned selE[4], selO[4];
#pragma unroll
  for (int k = 0; k < 4; ++k) {
    selE[k] = (k == (jj >> 1)) ? ((jj & 1) ? 0x01000c0cu : 0x0c0c0100u) : 0x0c0c0c0cu;
    selO[k] = (k == (jj >> 1)) ? ((jj & 1) ? 0x03020c0cu : 0x0c0c0302u) : 0x0c0c0c0cu;
  }
  auto buildA = [&](unsigned pw, int odd) -> v4u {
    v4u a;
    if (odd) { a.x = __builtin_amdgcn_perm(pw, pw, selO[0]); a.y = __builtin_amdgcn_perm(pw, pw, selO[1]); a.z = __builtin_amdgcn_perm(pw, pw, selO[2]); a.w = __builtin_amdgcn_perm(pw, pw, selO[3]); }
    else     { a.x = __builtin_amdgcn_perm(pw, pw, selE[0]); a.y = __builtin_amdgcn_perm(pw, pw, selE[1]); a.z = __builtin_amdgcn_perm(pw, pw, selE[2]); a.w = __builtin_amdgcn_perm(pw, pw, selE[3]); }
    return a;
  };
  auto arrive = [&](int c) {
    FENCE();
    if (lane == 0) __hip_atomic_fetch_add((LAS unsigned*)(L + L_CNT + 4 * c), 1u, __ATOMIC_RELAXED, __HIP_MEMORY_SCOPE_WORKGROUP);
    FENCE();
  };
  auto await = [&](int c, unsigned n) {
    FENCE();
    for (int s = 0; s < LSPIN_CAP; ++s) {
      if (*(volatile LAS unsigned*)(L + L_CNT + 4 * c) >= n) break;
    }
    FENCE();
  };

#define GATHER(N, v, base, stride, tagv) GATHERX(N, v, base, stride, tagv, , 0u)
#define GATHERX(N, v, base, stride, tagv, EXTRA_LOAD, EXTRA_BAD)                                                \
  {                                                                                                             \
    unsigned pend_ = (1u << (N)) - 1u;                                                                          \
    for (int spin_ = 0; spin_ < SPIN_CAP; ++spin_) {                                                            \
      asm volatile("" ::: "memory");                                                                            \
      v4u t_[N];                                                                                                \
      EXTRA_LOAD;                                                                                               \
      _Pragma("unroll") for (int i_ = 0; i_ < (N); ++i_)                                                        \
        t_[i_] = __builtin_amdgcn_raw_buffer_load_b128(rs, ((pend_ >> i_) & 1u) ? (base) + i_ * (stride) : OOB, 0, 16); \
      _Pragma("unroll") for (int i_ = 0; i_ < (N); ++i_) {                                                      \
        const bool p_ = (pend_ >> i_) & 1u;                                                                     \
        v[i_].x = p_ ? t_[i_].x : v[i_].x; v[i_].y = p_ ? t_[i_].y : v[i_].y;                                   \
        v[i_].z = p_ ? t_[i_].z : v[i_].z; v[i_].w = p_ ? t_[i_].w : v[i_].w;                                   \
        if (bad4(v[i_], tagv) == 0) pend_ &= ~(1u << i_);                                                       \
      }                                                                                                         \
      if (__all((pend_ | (EXTRA_BAD)) == 0)) break;                                                             \
      __builtin_amdgcn_s_sleep(1);                                                                              \
    }                                                                                                           \
  }

  if (wave < 4) {
    const int w = wave;
    unsigned tag, epoch;
    {
      const u16* wkb = wuk + ((size_t)(64 * j + 16 * w + r) * 32 + h) * 128 + 8 * g;
      v4u wk[4];
#pragma unroll
      for (int ks = 0; ks < 4; ++ks) wk[ks] = ldnt(wkb + 32 * ks);
      const u16* xp = x + (r >> 3) * 512 + 128 * w + 32 * g;
      v2u xv[8];
#pragma unroll
      for (int k = 0; k < 8; ++k) xv[k] = *(const v2u*)(xp + ((4 * k + 4 * j) & 31));
      const u16* wqb = wq + (size_t)(128 * w + 32 * g) * 4096 + h * 128 + 8 * r;
      v4u wv[32];
#pragma unroll
      for (int i = 0; i < 32; ++i) wv[i] = ldnt(wqb + (size_t)((i + 4 * j) & 31) * 4096);
      epoch = __builtin_amdgcn_raw_buffer_load_b32(rs, vzero + W_EPOCH, 0, 16);
      FENCE();
      __builtin_amdgcn_s_barrier();
      FENCE();
      f32x4 acc = {0.f, 0.f, 0.f, 0.f};
#pragma unroll
      for (int i = 0; i < 32; ++i) acc = mfma(buildA(xv[i >> 2][(i >> 1) & 1], i & 1), wv[i], acc);
      STAMP(1);
      {
        const unsigned h01 = pk_bf16(acc.x, acc.y), h23 = pk_bf16(acc.z, acc.w);
        const unsigned l01 = pk_bf16(acc.x - bf_lo(h01), acc.y - bf_hi(h01)), l23 = pk_bf16(acc.z - bf_lo(h23), acc.w - bf_hi(h23));
        const int b = g >> 1, d0 = 8 * r + 4 * (g & 1);
        *(LAS v2u*)(L + L_P + (b + 4 * w) * 288 + d0 * 2) = v2u{h01, h23};
        *(LAS v2u*)(L + L_P + (b + 2 + 4 * w) * 288 + d0 * 2) = v2u{l01, l23};
      }
      arrive(C_S1);
      await(C_S1, 4);
      f32x4 qa = {0.f, 0.f, 0.f, 0.f};
#pragma unroll
      for (int ks = 0; ks < 4; ++ks) qa = mfma(wk[ks], LD128(L_P + r * 288 + ks * 64 + g * 16), qa);
#pragma unroll
      for (int e = 0; e < 4; ++e) { float v = qa[e]; v += dpp_ror<8>(v); v += dpp_ror<4>(v); v += dpp_ror<2>(v); qa[e] = v * (0.08838834764831845f * 1.4426950408889634f); }
      tag = (epoch + 1u) & TMASK;
      __builtin_amdgcn_raw_buffer_store_b128(tag4(qa, tag), rs, W_H2 + ((r * 32 + h) * 512 + 64 * j + 16 * w + 4 * g) * 4, 0, 16);
    }
    STAMP(2);
    arrive(C_PUB);
    await(C_ISS, 4);
    STAMP(3);
    v4u kf[32];
    {
      v4u v[8] = {};
      const unsigned rep = (xcd & 3) * 2 + ((slot >> 1) & 1);
      const unsigned base = W_H2 + (unsigned)((rep * 2 + ab) * 32 + hg * 16) * 2048u + (unsigned)tid * 16u;
      GATHER(8, v, base, 4096u, tag);
#pragma unroll
      for (int i = 0; i < 8; ++i) {
        const int head = 2 * i + (tid >> 7), c = 4 * (tid & 127);
        *(LAS v2u*)(L + L_Q + head * PITCH + c * 2) = v2u{pk_bf16(untag(v[i].x), untag(v[i].y)), pk_bf16(untag(v[i].z), untag(v[i].w))};
      }
    }
    STAMP(11);
    arrive(C_S2);
    await(C_KV, 4);
    STAMP(12);
#pragma unroll
    for (int ks = 0; ks < 16; ++ks) {
      kf[2 * ks] = LD128(L_KV + (32 * w + r) * PITCH + ks * 64 + g * 16);
      kf[2 * ks + 1] = LD128(L_KV + (32 * w + 16 + r) * PITCH + ks * 64 + g * 16);
    }
    v4u wvv[4];
    {
      const u16* wvb = wuv + ((size_t)(64 * j + 16 * w + 4 * g) * 32 + h) * 128 + 8 * r;
#pragma unroll
      for (int i = 0; i < 4; ++i) wvv[i] = ldnt(wvb + (size_t)i * 4096);
    }
    await(C_S2, 4);
    STAMP(4);
    {
      f32x4 s0 = {0.f, 0.f, 0.f, 0.f}, s1 = {0.f, 0.f, 0.f, 0.f};
      const int tq = r >> 2, tp = r & 3;
      v4u vf[32];
      {
        v4u qf[16];
#pragma unroll
        for (int ks = 0; ks < 16; ++ks) qf[ks] = LD128(L_Q + r * PITCH + ks * 64 + g * 16);
        FENCE();
#pragma unroll
        for (int ks = 0; ks < 16; ++ks) {
          s0 = mfma(kf[2 * ks], qf[ks], s0);
          s1 = mfma(kf[2 * ks + 1], qf[ks], s1);
#pragma unroll
          for (int e = 0; e < 2; ++e) {
            const int ct = ks >> 1, i = 2 * (ks & 1) + e;
            const int a0 = L_KV + (32 * i + 4 * g + tq) * PITCH + (128 * w + 16 * ct + 4 * tp) * 2;
            const v2u t0 = __builtin_bit_cast(v2u, __builtin_amdgcn_ds_read_tr16_b64_v4i16((LAS s16x4*)(L + a0)));
            const v2u t1 = __builtin_bit_cast(v2u, __builtin_amdgcn_ds_read_tr16_b64_v4i16((LAS s16x4*)(L + a0 + 16 * PITCH)));
            vf[4 * ct + i] = v4u{t0.x, t0.y, t1.x, t1.y};
          }
          __builtin_amdgcn_sched_barrier(0);
        }
      }
      FENCE();
      float mx = fmaxf(fmaxf(fmaxf(s0.x, s0.y), fmaxf(s0.z, s0.w)), fmaxf(fmaxf(s1.x, s1.y), fmaxf(s1.z, s1.w)));
      mx = x32_max(x16_max(mx));
      if (g == 0) *(LAS float*)(L + L_SM + (r * 4 + w) * 4) = mx;
      arrive(C_S3);
      await(C_S3, 4);
      const f32x4 m0 = LDF4(L_SM + r * 16);
      float M = fmaxf(fmaxf(m0.x, m0.y), fmaxf(m0.z, m0.w));
      f32x4 p0, p1;
#pragma unroll
      for (int e = 0; e < 4; ++e) { p0[e] = __builtin_amdgcn_exp2f(s0[e] - M); p1[e] = __builtin_amdgcn_exp2f(s1[e] - M); }
      *(LAS v4u*)(L + L_P + w * 1024 + lane * 16) = v4u{pk_bf16(p0.x, p0.y), pk_bf16(p0.z, p0.w), pk_bf16(p1.x, p1.y), pk_bf16(p1.z, p1.w)};
      float ls = ((p0.x + p0.y) + (p0.z + p0.w)) + ((p1.x + p1.y) + (p1.z + p1.w));
      ls = x32_add(x16_add(ls));
      if (g == 0) *(LAS float*)(L + L_SM + 256 + (r * 4 + w) * 4) = ls;
      arrive(C_S4);
      await(C_S4, 4);
      STAMP(5);
      v4u pf[4];
#pragma unroll
      for (int i = 0; i < 4; ++i) pf[i] = LD128(L_P + i * 1024 + lane * 16);
      unsigned lsb;
      float osc;
      {
        const f32x4 l0 = LDF4(L_SM + 256 + r * 16);
        const float lse = M + __builtin_amdgcn_logf((l0.x + l0.y) + (l0.z + l0.w));
        lsb = (__float_as_uint(lse) + 0x80u) & 0xffffff00u;
        osc = __builtin_amdgcn_exp2f(M - __uint_as_float(lsb));
      }
      const unsigned f1 = lsb >> 20, f3 = (lsb >> 8) & TMASK;
      const int stg = L_KV + w * (32 * 272);
#pragma unroll
      for (int ct = 0; ct < 8; ++ct) {
        f32x4 o = {0.f, 0.f, 0.f, 0.f};
#pragma unroll
        for (int i = 0; i < 4; ++i) o = mfma(vf[4 * ct + i], pf[i], o);
        o *= osc;
        *(LAS v4u*)(L + stg + ((ct >> 2) * 16 + r) * 272 + ((ct & 3) * 4 + g) * 16) = v4u{tagw(o.x, tag), tagw(o.y, f1), tagw(o.z, tag), tagw(o.w, f3)};
      }
      FENCE();
#pragma unroll
      for (int k = 0; k < 8; ++k) {
        const v4u d = LD128(stg + (4 * k + g) * 272 + r * 16);
        __builtin_amdgcn_raw_buffer_store_b128(d, rs, W_H3 + (((ab * 64 + as) * 2 + hg) * 32 + 8 * w + k) * 1024 + lane * 16, 0, 16);
      }
    }
    arrive(C_ST);
    STAMP(6);
    await(C_KVH, 4);
    {
      const int b = w >> 1;
      const int c4 = tid & 15, sg = (tid >> 4) & 7;
      v4u v[8] = {};
      const unsigned obase = W_H3 + (unsigned)(((b * 64 + sg) * 2 + (h >> 4)) * 32 + 4 * j) * 1024u + (h & 15) * 256u + c4 * 16u;
      {
        unsigned pend = 0xffu;
        for (int spin = 0; spin < SPIN_CAP; ++spin) {
          asm volatile("" ::: "memory");
          v4u t[8];
#pragma unroll
          for (int i = 0; i < 8; ++i) t[i] = __builtin_amdgcn_raw_buffer_load_b128(rs, ((pend >> i) & 1u) ? obase + i * (8u * 65536u) : OOB, 0, 16);
          unsigned badm = 0;
#pragma unroll
          for (int i = 0; i < 8; ++i) badm |= ((((t[i].x ^ tag) | (t[i].z ^ tag)) & TMASK) ? 1u : 0u) << i;
          badm |= (unsigned)__builtin_amdgcn_update_dpp(0, (int)badm, 0x128, 0xf, 0xf, false);
          badm |= (unsigned)__builtin_amdgcn_update_dpp(0, (int)badm, 0x124, 0xf, 0xf, false);
          badm |= (unsigned)__builtin_amdgcn_update_dpp(0, (int)badm, 0x122, 0xf, 0xf, false);
          badm |= (unsigned)__builtin_amdgcn_update_dpp(0, (int)badm, 0x121, 0xf, 0xf, false);
#pragma unroll
          for (int i = 0; i < 8; ++i) {
            const bool p = (pend >> i) & 1u;
            v[i].x = p ? t[i].x : v[i].x; v[i].y = p ? t[i].y : v[i].y; v[i].z = p ? t[i].z : v[i].z; v[i].w = p ? t[i].w : v[i].w;
          }
          pend &= badm;
          if (__all(pend == 0)) break;
          __builtin_amdgcn_s_sleep(H3_SLEEP);
        }
      }
      STAMP(7);
      float ls[8];
      float Mw = -3.0e38f;
#pragma unroll
      for (int i = 0; i < 8; ++i) { ls[i] = __uint_as_float(((v[i].y & TMASK) << 20) | ((v[i].w & TMASK) << 8)); Mw = fmaxf(Mw, ls[i]); }
      Mw = x32_max(x16_max(Mw));
      f32x4 o4 = {0.f, 0.f, 0.f, 0.f};
      float Lw = 0.f;
#pragma unroll
      for (int i = 0; i < 8; ++i) {
        const float ws_ = __builtin_amdgcn_exp2f(ls[i] - Mw);
        Lw += ws_;
        o4.x += ws_ * untag(v[i].x); o4.y += ws_ * untag(v[i].y); o4.z += ws_ * untag(v[i].z); o4.w += ws_ * untag(v[i].w);
      }
#pragma unroll
      for (int e2 = 0; e2 < 4; ++e2) o4[e2] = x32_add(x16_add(o4[e2]));
      Lw = x32_add(x16_add(Lw));
      if (g == 0) *(LAS f32x4*)(L + L_OL + w * 256 + c4 * 16) = o4;
      if (lane == 0) *(LAS v2u*)(L + L_SM + w * 8) = v2u{__float_as_uint(Mw), __float_as_uint(Lw)};
    }
    arrive(C_S5);
    await(C_S5, 4);
    {
      const int b = r >> 3;
      const f32x4 oa = LDF4(L_OL + (2 * b) * 256 + (16 * w + 4 * g) * 4), ob = LDF4(L_OL + (2 * b + 1) * 256 + (16 * w + 4 * g) * 4);
      const f32x4 ml = LDF4(L_SM + b * 16);
      const float Mb = fmaxf(ml.x, ml.z);
      const float ea = __builtin_amdgcn_exp2f(ml.x - Mb), eb = __builtin_amdgcn_exp2f(ml.z - Mb);
      const float inv = 1.f / (ml.y * ea + ml.w * eb);
      const f32x4 ov = oa * (ea * inv) + ob * (eb * inv);
      const unsigned h01 = pk_bf16(ov.x, ov.y), h23 = pk_bf16(ov.z, ov.w);
      const unsigned l01 = pk_bf16(ov.x - bf_lo(h01), ov.y - bf_hi(h01)), l23 = pk_bf16(ov.z - bf_lo(h23), ov.w - bf_hi(h23));
      f32x4 c0 = {0.f, 0.f, 0.f, 0.f}, c1 = {0.f, 0.f, 0.f, 0.f};
      c0 = mfma(buildA(h01, 0), wvv[0], c0); c1 = mfma(buildA(l01, 0), wvv[0], c1);
      c0 = mfma(buildA(h01, 1), wvv[1], c0); c1 = mfma(buildA(l01, 1), wvv[1], c1);
      c0 = mfma(buildA(h23, 0), wvv[2], c0); c1 = mfma(buildA(l23, 0), wvv[2], c1);
      c0 = mfma(buildA(h23, 1), wvv[3], c0); c1 = mfma(buildA(l23, 1), wvv[3], c1);
      *(LAS f32x4*)(L + L_CTX + w * 1024 + (g >> 1) * 512 + (8 * r + 4 * (g & 1)) * 4) = c0 + c1;
    }
    arrive(C_CTX);
    STAMP(8);
    if ((bid & 3) == 0) {
      const int oc = bid >> 2;
      v4u q[4] = {};
      const unsigned qbase = W_H4 + (unsigned)(64 * w + (lane >> 2)) * 4096u + oc * 64u + (lane & 3) * 16u;
      GATHER(4, q, qbase, 16u * 4096u, tag);
      STAMP(9);
      f32x4 t4;
#pragma unroll
      for (int e2 = 0; e2 < 4; ++e2) {
        float t = (untag(q[0][e2]) + untag(q[1][e2])) + (untag(q[2][e2]) + untag(q[3][e2]));
        t += dpp_ror<8>(t); t += dpp_ror<4>(t);
        t4[e2] = x32_add(x16_add(t));
      }
      if (lane < 4) *(LAS f32x4*)(L + L_SM + 256 + w * 64 + lane * 16) = t4;
      arrive(C_FIN);
      if (w == 0) {
        await(C_FIN, 4);
        if (lane < 4) {
          const f32x4 o = (LDF4(L_SM + 256 + lane * 16) + LDF4(L_SM + 320 + lane * 16)) + (LDF4(L_SM + 384 + lane * 16) + LDF4(L_SM + 448 + lane * 16));
          *(v2u*)(out + oc * 16 + lane * 4) = v2u{pk_bf16(o.x, o.y), pk_bf16(o.z, o.w)};
          if (bid == 0 && lane == 0) __builtin_amdgcn_raw_buffer_store_b32(epoch + 1u, rs, W_EPOCH, 0, 16);
        }
      }
    }
    STAMP(10);
    STAMP_FLUSH();
  } else {
    const int w = wave - 4;
    if (w == 0 && lane < 16) *(LAS unsigned*)(L + L_CNT + 4 * lane) = 0u;
    asm volatile("s_waitcnt lgkmcnt(0)" ::: "memory");
    FENCE();
    __builtin_amdgcn_s_barrier();
    FENCE();
    const v4u gx0 = *(const v4u*)(x + lane * 8), gx1 = *(const v4u*)(x + 512 + lane * 8), gwv = *(const v4u*)(gw + lane * 8);
    const unsigned epoch = __builtin_amdgcn_raw_buffer_load_b32(rs, vzero + W_EPOCH, 0, 16);
    await(C_PUB, 4);
    const u16* src = ckv + ((size_t)ab * 8192 + as * 128 + w * 32) * 512 + lane * 8;
#pragma unroll
    for (int i = 0; i < ISS_AT; ++i)
      __builtin_amdgcn_global_load_lds((const void*)(src + i * 512), (LAS void*)(L + L_KV + (w * 32 + i) * PITCH), 16, 0, 2);
    FENCE();
    if (lane == 0) asm volatile("ds_add_u32 %0, %1" ::"v"((unsigned)(L_CNT + 4 * C_ISS)), "v"(1u) : "memory");
    FENCE();
#pragma unroll
    for (int i = ISS_AT; i < 32; ++i)
      __builtin_amdgcn_global_load_lds((const void*)(src + i * 512), (LAS void*)(L + L_KV + (w * 32 + i) * PITCH), 16, 0, 2);
    FENCE();
    __builtin_amdgcn_s_waitcnt(0x0f70);
    arrive(C_KV);
    STAMP(1);
    v4u fv[8]; v4u fx;
    const bool ffn1 = (w < 2) && (bid < 224);
    const int cb = bid % 14, rb = bid / 14;
    await(C_S2, 4);
    if (ffn1) {
      const u16* fb = (w == 0 ? wg : wu) + (size_t)(32 * rb + 8 * g) * 1792 + 128 * cb + 8 * r;
#pragma unroll
      for (int i = 0; i < 8; ++i) fv[i] = ldnt(fb + (size_t)i * 1792);
      fx = *(const v4u*)(x + (r >> 3) * 512 + 32 * rb + 8 * g);
    }
    v4u wov[34];
    const u16* wob = wo + ((size_t)h * 128 + 32 * g) * 512 + 128 * w + 8 * r;
#pragma unroll
    for (int i = 0; i < WO_A; ++i) wov[i] = ldnt(wob + ((i + 4 * j) & 31) * 512);
    {
      const int u = bid < 224 ? bid : 0;
      const u16* wdb = wd + (size_t)(8 * u + 2 * g) * 512 + 128 * w + 8 * r;
      wov[32] = ldnt(wdb);
      wov[33] = ldnt(wdb + 512);
    }
    FENCE();
    const unsigned tag = (epoch + 1u) & TMASK;
    if (ffn1) {
      f32x4 acc = {0.f, 0.f, 0.f, 0.f};
#pragma unroll
      for (int i = 0; i < 8; ++i) acc = mfma(buildA(fx[i >> 1], i & 1), fv[i], acc);
      const unsigned u = 16 * cb + r;
      __builtin_amdgcn_raw_buffer_store_b128(tag4(acc, tag), rs, W_HF + u * 2048 + ((w * 16 + rb) * 4 + g) * 16, 0, 16);
    }
    float gate0, gate1;
    {
      float s0 = 0.f, s1 = 0.f;
#pragma unroll
      for (int k = 0; k < 4; ++k) {
        s0 += bf_lo(gx0[k]) * bf_lo(gwv[k]) + bf_hi(gx0[k]) * bf_hi(gwv[k]);
        s1 += bf_lo(gx1[k]) * bf_lo(gwv[k]) + bf_hi(gx1[k]) * bf_hi(gwv[k]);
      }
      s0 = wave_sum(s0); s1 = wave_sum(s1);
      gate0 = 1.f / (1.f + __expf(-s0));
      gate1 = 1.f / (1.f + __expf(-s1));
    }
    STAMP(2);
    {
      STAMP(4);
      await(C_ST, 4);
      STAMP(5);
#pragma unroll
      for (int i = WO_A; i < 32; ++i) wov[i] = ldnt(wob + ((i + 4 * j) & 31) * 512);
    }
    arrive(C_KVH);
    STAMP(6);
    unsigned hp = 0u, lp = 0u;
    if (bid < 224) {
      v4u ff[2] = {};
      const unsigned fbase = W_HF + bid * 2048 + lane * 16;
      GATHER(2, ff, fbase, 1024u, tag);
      const v4u f0 = ff[0], f1 = ff[1];
      f32x4 hv;
      const float og = 1.f - (((lane >> 1) & 1) ? gate1 : gate0);
#pragma unroll
      for (int e2 = 0; e2 < 4; ++e2) {
        float t = untag(f0[e2]); t += dpp_ror<8>(t); t += dpp_ror<4>(t); t = x32_add(x16_add(t));
        float u = untag(f1[e2]); u += dpp_ror<8>(u); u += dpp_ror<4>(u); u = x32_add(x16_add(u));
        const float a2 = 2.f * 0.7978845608028654f * (t + 0.044715f * t * t * t);
        hv[e2] = t / (1.f + __expf(-a2)) * u * og;
      }
      const int srcl = (r >> 3) * 2 + (g >> 1);
      const float a0 = __shfl(hv.x, srcl), a1 = __shfl(hv.y, srcl), a2_ = __shfl(hv.z, srcl), a3 = __shfl(hv.w, srcl);
      const float h0 = (g & 1) ? a2_ : a0, h1 = (g & 1) ? a3 : a1;
      hp = pk_bf16(h0, h1);
      lp = pk_bf16(h0 - bf_lo(hp), h1 - bf_hi(hp));
    }
    f32x4 acc = {0.f, 0.f, 0.f, 0.f};
    acc = mfma(buildA(hp, 0), wov[32], acc);
    acc = mfma(buildA(hp, 1), wov[33], acc);
    acc = mfma(buildA(lp, 0), wov[32], acc);
    acc = mfma(buildA(lp, 1), wov[33], acc);
    STAMP(7);
    await(C_CTX, 4);
    STAMP(8);
    {
      const int b = r >> 3;
      const float gb = b ? gate1 : gate0;
      const int off = L_CTX + b * 512 + (32 * g + 4 * jj) * 4;
      const f32x4 cs = ((LDF4(off) + LDF4(off + 1024)) + (LDF4(off + 2048) + LDF4(off + 3072))) * gb;
      *(LAS v2u*)(L + L_PK + w * 512 + b * 256 + (16 * g + 2 * jj) * 4) = v2u{pk_bf16(cs.x, cs.y), pk_bf16(cs.z, cs.w)};
      FENCE();
      v2u pk[8];
#pragma unroll
      for (int k = 0; k < 8; ++k) pk[k] = *(const LAS v2u*)(L + L_PK + w * 512 + b * 256 + (16 * g + ((2 * k + 2 * j) & 15)) * 4);
#pragma unroll
      for (int i = 0; i < 32; ++i) acc = mfma(buildA(pk[i >> 2][(i >> 1) & 1], i & 1), wov[i], acc);
    }
    const unsigned cons = (g >> 1) * 128 + 32 * w + 2 * r + (g & 1);
    __builtin_amdgcn_raw_buffer_store_b128(tag4(acc, tag), rs, W_H4 + bid * 4096 + cons * 16, 0, 16);
    STAMP(9);
    STAMP_FLUSH();
  }
}
}

extern "C" void kernel_launch(void* const* d_in, const int* in_sizes, int n_in, void* d_out, int out_size, void* d_ws, size_t ws_size,
                              hipStream_t stream) {
  (void)in_sizes; (void)n_in; (void)out_size; (void)ws_size;
  (void)hipFuncSetAttribute((const void*)mk, hipFuncAttributeMaxDynamicSharedMemorySize, L_ALLOC);
  mk<<<256, 512, L_ALLOC, stream>>>((const u16*)d_in[0], (const u16*)d_in[1], (const u16*)d_in[2], (const u16*)d_in[3], (const u16*)d_in[4],
                                  (const u16*)d_in[5], (const u16*)d_in[7], (const u16*)d_in[8], (const u16*)d_in[9], (const u16*)d_in[10],
                                  (u16*)d_out, (unsigned char*)d_ws);
}
```

```cpp
#define TTH 27.0f
#include <hip/hip_runtime.h>
#include <hip/hip_bf16.h>
#include <stdint.h>

typedef unsigned short u16;
typedef unsigned v4u __attribute__((ext_vector_type(4)));
typedef unsigned v2u __attribute__((ext_vector_type(2)));
typedef float f32x4 __attribute__((ext_vector_type(4)));
typedef short s16x4 __attribute__((ext_vector_type(4)));
typedef __bf16 bf16x8 __attribute__((ext_vector_type(8)));
#define LAS __attribute__((address_space(3)))
#define GAS __attribute__((address_space(1)))

namespace {
constexpr int PITCH = 1056;
constexpr int L_KV = 0;
constexpr int L_Q = 128 * PITCH;
constexpr int L_P = L_Q + 16 * PITCH;
constexpr int L_SM = L_P + 4608;
constexpr int L_OL = L_Q;
constexpr int L_CTX = L_OL + 1024;
constexpr int L_PK = L_CTX + 4096;
constexpr int L_CNT = L_SM + 512;
constexpr int L_END = L_CNT + 64;
static_assert(L_END <= 160 * 1024, "LDS");
enum { C_S1 = 0, C_S2, C_KV, C_S3, C_S4, C_S5, C_CTX, C_ST, C_FIN, C_KVH, C_PUB, C_ISS };
constexpr unsigned OOB = 0xfffffff0u;

constexpr unsigned W_EPOCH = 0;
constexpr unsigned W_H2 = 4096;
constexpr unsigned W_HF = W_H2 + 8 * 2 * 32 * 512 * 4;
constexpr unsigned W_H4 = W_HF + 224 * 2048;
constexpr unsigned W_H3 = W_H4 + 256 * 256 * 16;
constexpr unsigned W_DBG = W_H3 + 32 * 8 * 2 * 64 * 64 * 4;
constexpr unsigned W_END = W_DBG + 256 * 2 * 16 * 8;
#define H3_SLEEP 8
#define H2_DELAY 20
#define H3_GATE 1
#define ISS_AT 20
#define TLBW 0
#define KVH_N 20
#define WO_A 4
constexpr int SPIN_CAP = 1 << 15;
constexpr int LSPIN_CAP = 1 << 22;

__device__ __forceinline__ f32x4 mfma(v4u a, v4u b, f32x4 c) {
  return __builtin_amdgcn_mfma_f32_16x16x32_bf16(__builtin_bit_cast(bf16x8, a), __builtin_bit_cast(bf16x8, b), c, 0, 0, 0);
}
__device__ __forceinline__ float bf_lo(unsigned w) { return __uint_as_float(w << 16); }
__device__ __forceinline__ float bf_hi(unsigned w) { return __uint_as_float(w & 0xffff0000u); }
__device__ __forceinline__ unsigned pk_bf16(float a, float b) {
  typedef __bf16 bf16x2 __attribute__((ext_vector_type(2)));
  typedef float f32x2 __attribute__((ext_vector_type(2)));
  f32x2 v = {a, b};
  bf16x2 r = __builtin_convertvector(v, bf16x2);
  return __builtin_bit_cast(unsigned, r);
}
constexpr unsigned TMASK = 0xfffu;
__device__ __forceinline__ unsigned tagw(float v, unsigned tag) { return ((__float_as_uint(v) + 0x800u) & ~TMASK) | tag; }
__device__ __forceinline__ float untag(unsigned w) { return __uint_as_float(w & ~TMASK); }
__device__ __forceinline__ v4u tag4(f32x4 v, unsigned tag) { return v4u{tagw(v.x, tag), tagw(v.y, tag), tagw(v.z, tag), tagw(v.w, tag)}; }
__device__ __forceinline__ unsigned bad4(v4u w, unsigned tag) { return ((w.x ^ tag) | (w.y ^ tag) | (w.z ^ tag) | (w.w ^ tag)) & TMASK; }
template <int N> __device__ __forceinline__ float dpp_ror(float v) {
  return __int_as_float(__builtin_amdgcn_update_dpp(0, __float_as_int(v), 0x120 + N, 0xf, 0xf, false));
}
__device__ __forceinline__ float x16_add(float v) { auto p = __builtin_amdgcn_permlane16_swap(__float_as_uint(v), __float_as_uint(v), false, false); return __uint_as_float(p[0]) + __uint_as_float(p[1]); }
__device__ __forceinline__ float x32_add(float v) { auto p = __builtin_amdgcn_permlane32_swap(__float_as_uint(v), __float_as_uint(v), false, false); return __uint_as_float(p[0]) + __uint_as_float(p[1]); }
__device__ __forceinline__ float x16_max(float v) { auto p = __builtin_amdgcn_permlane16_swap(__float_as_uint(v), __float_as_uint(v), false, false); return fmaxf(__uint_as_float(p[0]), __uint_as_float(p[1])); }
__device__ __forceinline__ float x32_max(float v) { auto p = __builtin_amdgcn_permlane32_swap(__float_as_uint(v), __float_as_uint(v), false, false); return fmaxf(__uint_as_float(p[0]), __uint_as_float(p[1])); }
__device__ __forceinline__ float wave_sum(float v) { v += dpp_ror<8>(v); v += dpp_ror<4>(v); v += dpp_ror<2>(v); v += dpp_ror<1>(v); v = x16_add(v); return x32_add(v); }
__device__ __forceinline__ float wave_max(float v) { v = fmaxf(v, dpp_ror<8>(v)); v = fmaxf(v, dpp_ror<4>(v)); v = fmaxf(v, dpp_ror<2>(v)); v = fmaxf(v, dpp_ror<1>(v)); v = x16_max(v); return x32_max(v); }
__device__ __forceinline__ v4u ldnt(const u16* p) { return __builtin_nontemporal_load((const v4u*)p); }

#define LD128(off) (*(const LAS v4u*)(L + (off)))
#define LDF4(off) (*(const LAS f32x4*)(L + (off)))
#define FENCE() do { __builtin_amdgcn_sched_barrier(0); asm volatile("" ::: "memory"); } while (0)

#define STAMP(i)
#define STAMP_FLUSH()
#define L_ALLOC L_END

__global__ __launch_bounds__(512, 2) void mk(const u16* __restrict__ x, const u16* __restrict__ ckv, const u16* __restrict__ wq,
                                             const u16* __restrict__ wuk, const u16* __restrict__ wuv, const u16* __restrict__ wo,
                                             const u16* __restrict__ wg, const u16* __restrict__ wu, const u16* __restrict__ wd,
                                             const u16* __restrict__ gw, u16* __restrict__ out, unsigned char* ws) {
  extern __shared__ __attribute__((aligned(16))) unsigned char lds[];
  LAS unsigned char* L = (LAS unsigned char*)lds;
  const int tid = threadIdx.x, lane = tid & 63, wave = __builtin_amdgcn_readfirstlane(tid >> 6);
  const int bid = blockIdx.x, xcd = bid & 7, slot = bid >> 3;
  const int h = xcd * 4 + (slot >> 3), j = slot & 7;
  const int ab = xcd >> 2, as = (xcd & 3) * 16 + (slot >> 1), hg = slot & 1;
  const int g = lane >> 4, r = lane & 15, jj = r & 7;
  const auto rs = __builtin_amdgcn_make_buffer_rsrc(ws, 0, W_END, 0x00020000);
  STAMP(0);
  const unsigned long long tstart_ = __builtin_amdgcn_s_memrealtime();
  int vzero = 0;
  asm volatile("" : "+v"(vzero));
  unsigned selE[4], selO[4];
#pragma unroll
  for (int k = 0; k < 4; ++k) {
    selE[k] = (k == (jj >> 1)) ? ((jj & 1) ? 0x01000c0cu : 0x0c0c0100u) : 0x0c0c0c0cu;
    selO[k] = (k == (jj >> 1)) ? ((jj & 1) ? 0x03020c0cu : 0x0c0c0302u) : 0x0c0c0c0cu;
  }
  auto buildA = [&](unsigned pw, int odd) -> v4u {
    v4u a;
    if (odd) { a.x = __builtin_amdgcn_perm(pw, pw, selO[0]); a.y = __builtin_amdgcn_perm(pw, pw, selO[1]); a.z = __builtin_amdgcn_perm(pw, pw, selO[2]); a.w = __builtin_amdgcn_perm(pw, pw, selO[3]); }
    else     { a.x = __builtin_amdgcn_perm(pw, pw, selE[0]); a.y = __builtin_amdgcn_perm(pw, pw, selE[1]); a.z = __builtin_amdgcn_perm(pw, pw, selE[2]); a.w = __builtin_amdgcn_perm(pw, pw, selE[3]); }
    return a;
  };
  auto arrive = [&](int c) {
    FENCE();
    if (lane == 0) __hip_atomic_fetch_add((LAS unsigned*)(L + L_CNT + 4 * c), 1u, __ATOMIC_RELAXED, __HIP_MEMORY_SCOPE_WORKGROUP);
    FENCE();
  };
  auto await = [&](int c, unsigned n) {
    FENCE();
    for (int s = 0; s < LSPIN_CAP; ++s) {
      if (*(volatile LAS unsigned*)(L + L_CNT + 4 * c) >= n) break;
    }
    FENCE();
  };

#define GATHER(N, v, base, stride, tagv) GATHERX(N, v, base, stride, tagv, , 0u)
#define GATHERX(N, v, base, stride, tagv, EXTRA_LOAD, EXTRA_BAD)                                                \
  {                                                                                                             \
    unsigned pend_ = (1u << (N)) - 1u;                                                                          \
    for (int spin_ = 0; spin_ < SPIN_CAP; ++spin_) {                                                            \
      asm volatile("" ::: "memory");                                                                            \
      v4u t_[N];                                                                                                \
      EXTRA_LOAD;                                                                                               \
      _Pragma("unroll") for (int i_ = 0; i_ < (N); ++i_)                                                        \
        t_[i_] = __builtin_amdgcn_raw_buffer_load_b128(rs, ((pend_ >> i_) & 1u) ? (base) + i_ * (stride) : OOB, 0, 16); \
      _Pragma("unroll") for (int i_ = 0; i_ < (N); ++i_) {                                                      \
        const bool p_ = (pend_ >> i_) & 1u;                                                                     \
        v[i_].x = p_ ? t_[i_].x : v[i_].x; v[i_].y = p_ ? t_[i_].y : v[i_].y;                                   \
        v[i_].z = p_ ? t_[i_].z : v[i_].z; v[i_].w = p_ ? t_[i_].w : v[i_].w;                                   \
        if (bad4(v[i_], tagv) == 0) pend_ &= ~(1u << i_);                                                       \
      }                                                                                                         \
      if (__all((pend_ | (EXTRA_BAD)) == 0)) break;                                                             \
      __builtin_amdgcn_s_sleep(1);                                                                              \
    }                                                                                                           \
  }

  if (wave < 4) {
    const int w = wave;
    unsigned tag, epoch;
    {
      const u16* wkb = wuk + ((size_t)(64 * j + 16 * w + r) * 32 + h) * 128 + 8 * g;
      v4u wk[4];
#pragma unroll
      for (int ks = 0; ks < 4; ++ks) wk[ks] = ldnt(wkb + 32 * ks);
      const u16* xp = x + (r >> 3) * 512 + 128 * w + 32 * g;
      v2u xv[8];
#pragma unroll
      for (int k = 0; k < 8; ++k) xv[k] = *(const v2u*)(xp + ((4 * k + 4 * j) & 31));
      const u16* wqb = wq + (size_t)(128 * w + 32 * g) * 4096 + h * 128 + 8 * r;
      v4u wv[32];
#pragma unroll
      for (int i = 0; i < 32; ++i) wv[i] = ldnt(wqb + (size_t)((i + 4 * j) & 31) * 4096);
      epoch = __builtin_amdgcn_raw_buffer_load_b32(rs, vzero + W_EPOCH, 0, 16);
      FENCE();
      __builtin_amdgcn_s_barrier();
      FENCE();
      f32x4 acc = {0.f, 0.f, 0.f, 0.f};
#pragma unroll
      for (int i = 0; i < 32; ++i) acc = mfma(buildA(xv[i >> 2][(i >> 1) & 1], i & 1), wv[i], acc);
      STAMP(1);
      {
        const unsigned h01 = pk_bf16(acc.x, acc.y), h23 = pk_bf16(acc.z, acc.w);
        const unsigned l01 = pk_bf16(acc.x - bf_lo(h01), acc.y - bf_hi(h01)), l23 = pk_bf16(acc.z - bf_lo(h23), acc.w - bf_hi(h23));
        const int b = g >> 1, d0 = 8 * r + 4 * (g & 1);
        *(LAS v2u*)(L + L_P + (b + 4 * w) * 288 + d0 * 2) = v2u{h01, h23};
        *(LAS v2u*)(L + L_P + (b + 2 + 4 * w) * 288 + d0 * 2) = v2u{l01, l23};
      }
      arrive(C_S1);
      await(C_S1, 4);
      f32x4 qa = {0.f, 0.f, 0.f, 0.f};
#pragma unroll
      for (int ks = 0; ks < 4; ++ks) qa = mfma(wk[ks], LD128(L_P + r * 288 + ks * 64 + g * 16), qa);
#pragma unroll
      for (int e = 0; e < 4; ++e) { float v = qa[e]; v += dpp_ror<8>(v); v += dpp_ror<4>(v); v += dpp_ror<2>(v); qa[e] = v * (0.08838834764831845f * 1.4426950408889634f); }
      tag = (epoch + 1u) & TMASK;
      __builtin_amdgcn_raw_buffer_store_b128(tag4(qa, tag), rs, W_H2 + ((r * 32 + h) * 512 + 64 * j + 16 * w + 4 * g) * 4, 0, 16);
    }
    STAMP(2);
    arrive(C_PUB);
    await(C_ISS, 4);
    STAMP(3);
    v4u kf[32];
    {
      v4u v[8] = {};
      const unsigned rep = (xcd & 3) * 2 + ((slot >> 1) & 1);
      const unsigned base = W_H2 + (unsigned)((rep * 2 + ab) * 32 + hg * 16) * 2048u + (unsigned)tid * 16u;
      GATHER(8, v, base, 4096u, tag);
#pragma unroll
      for (int i = 0; i < 8; ++i) {
        const int head = 2 * i + (tid >> 7), c = 4 * (tid & 127);
        *(LAS v2u*)(L + L_Q + head * PITCH + c * 2) = v2u{pk_bf16(untag(v[i].x), untag(v[i].y)), pk_bf16(untag(v[i].z), untag(v[i].w))};
      }
    }
    STAMP(11);
    arrive(C_S2);
    await(C_KV, 4);
    STAMP(12);
#pragma unroll
    for (int ks = 0; ks < 16; ++ks) {
      kf[2 * ks] = LD128(L_KV + (32 * w + r) * PITCH + ks * 64 + g * 16);
      kf[2 * ks + 1] = LD128(L_KV + (32 * w + 16 + r) * PITCH + ks * 64 + g * 16);
    }
    v4u wvv[4];
    {
      const u16* wvb = wuv + ((size_t)(64 * j + 16 * w + 4 * g) * 32 + h) * 128 + 8 * r;
#pragma unroll
      for (int i = 0; i < 4; ++i) wvv[i] = ldnt(wvb + (size_t)i * 4096);
    }
    await(C_S2, 4);
    STAMP(4);
    {
      f32x4 s0 = {0.f, 0.f, 0.f, 0.f}, s1 = {0.f, 0.f, 0.f, 0.f};
      const int tq = r >> 2, tp = r & 3;
      v4u vf[32];
      {
        v4u qf[16];
#pragma unroll
        for (int ks = 0; ks < 16; ++ks) qf[ks] = LD128(L_Q + r * PITCH + ks * 64 + g * 16);
        FENCE();
#pragma unroll
        for (int ks = 0; ks < 16; ++ks) {
          s0 = mfma(kf[2 * ks], qf[ks], s0);
          s1 = mfma(kf[2 * ks + 1], qf[ks], s1);
#pragma unroll
          for (int e = 0; e < 2; ++e) {
            const int ct = ks >> 1, i = 2 * (ks & 1) + e;
            const int a0 = L_KV + (32 * i + 4 * g + tq) * PITCH + (128 * w + 16 * ct + 4 * tp) * 2;
            const v2u t0 = __builtin_bit_cast(v2u, __builtin_amdgcn_ds_read_tr16_b64_v4i16((LAS s16x4*)(L + a0)));
            const v2u t1 = __builtin_bit_cast(v2u, __builtin_amdgcn_ds_read_tr16_b64_v4i16((LAS s16x4*)(L + a0 + 16 * PITCH)));
            vf[4 * ct + i] = v4u{t0.x, t0.y, t1.x, t1.y};
          }
          __builtin_amdgcn_sched_barrier(0);
        }
      }
      FENCE();
      float mx = fmaxf(fmaxf(fmaxf(s0.x, s0.y), fmaxf(s0.z, s0.w)), fmaxf(fmaxf(s1.x, s1.y), fmaxf(s1.z, s1.w)));
      mx = x32_max(x16_max(mx));
      if (g == 0) *(LAS float*)(L + L_SM + (r * 4 + w) * 4) = mx;
      arrive(C_S3);
      await(C_S3, 4);
      const f32x4 m0 = LDF4(L_SM + r * 16);
      float M = fmaxf(fmaxf(m0.x, m0.y), fmaxf(m0.z, m0.w));
      f32x4 p0, p1;
#pragma unroll
      for (int e = 0; e < 4; ++e) { p0[e] = __builtin_amdgcn_exp2f(s0[e] - M); p1[e] = __builtin_amdgcn_exp2f(s1[e] - M); }
      *(LAS v4u*)(L + L_P + w * 1024 + lane * 16) = v4u{pk_bf16(p0.x, p0.y), pk_bf16(p0.z, p0.w), pk_bf16(p1.x, p1.y), pk_bf16(p1.z, p1.w)};
      float ls = ((p0.x + p0.y) + (p0.z + p0.w)) + ((p1.x + p1.y) + (p1.z + p1.w));
      ls = x32_add(x16_add(ls));
      if (g == 0) *(LAS float*)(L + L_SM + 256 + (r * 4 + w) * 4) = ls;
      arrive(C_S4);
      await(C_S4, 4);
      STAMP(5);
      v4u pf[4];
#pragma unroll
      for (int i = 0; i < 4; ++i) pf[i] = LD128(L_P + i * 1024 + lane * 16);
      unsigned lsb;
      float osc;
      {
        const f32x4 l0 = LDF4(L_SM + 256 + r * 16);
        const float lse = M + __builtin_amdgcn_logf((l0.x + l0.y) + (l0.z + l0.w));
        lsb = (__float_as_uint(lse) + 0x80u) & 0xffffff00u;
        osc = __builtin_amdgcn_exp2f(M - __uint_as_float(lsb));
      }
      const unsigned f1 = lsb >> 20, f3 = (lsb >> 8) & TMASK;
      const int stg = L_KV + w * (32 * 272);
#pragma unroll
      for (int ct = 0; ct < 8; ++ct) {
        f32x4 o = {0.f, 0.f, 0.f, 0.f};
#pragma unroll
        for (int i = 0; i < 4; ++i) o = mfma(vf[4 * ct + i], pf[i], o);
        o *= osc;
        *(LAS v4u*)(L + stg + ((ct >> 2) * 16 + r) * 272 + ((ct & 3) * 4 + g) * 16) = v4u{tagw(o.x, tag), tagw(o.y, f1), tagw(o.z, tag), tagw(o.w, f3)};
      }
      FENCE();
#pragma unroll
      for (int k = 0; k < 8; ++k) {
        const v4u d = LD128(stg + (4 * k + g) * 272 + r * 16);
        __builtin_amdgcn_raw_buffer_store_b128(d, rs, W_H3 + (((ab * 64 + as) * 2 + hg) * 32 + 8 * w + k) * 1024 + lane * 16, 0, 16);
      }
    }
    arrive(C_ST);
    STAMP(6);
    await(C_KVH, 4);
    {
      const int b = w >> 1;
      const int c4 = tid & 15, sg = (tid >> 4) & 7;
      v4u v[8] = {};
      const unsigned obase = W_H3 + (unsigned)(((b * 64 + sg) * 2 + (h >> 4)) * 32 + 4 * j) * 1024u + (h & 15) * 256u + c4 * 16u;
      {
        unsigned pend = 0xffu;
        for (int spin = 0; spin < SPIN_CAP; ++spin) {
          asm volatile("" ::: "memory");
          v4u t[8];
#pragma unroll
          for (int i = 0; i < 8; ++i) t[i] = __builtin_amdgcn_raw_buffer_load_b128(rs, ((pend >> i) & 1u) ? obase + i * (8u * 65536u) : OOB, 0, 16);
          unsigned badm = 0;
#pragma unroll
          for (int i = 0; i < 8; ++i) badm |= ((((t[i].x ^ tag) | (t[i].z ^ tag)) & TMASK) ? 1u : 0u) << i;
          badm |= (unsigned)__builtin_amdgcn_update_dpp(0, (int)badm, 0x128, 0xf, 0xf, false);
          badm |= (unsigned)__builtin_amdgcn_update_dpp(0, (int)badm, 0x124, 0xf, 0xf, false);
          badm |= (unsigned)__builtin_amdgcn_update_dpp(0, (int)badm, 0x122, 0xf, 0xf, false);
          badm |= (unsigned)__builtin_amdgcn_update_dpp(0, (int)badm, 0x121, 0xf, 0xf, false);
#pragma unroll
          for (int i = 0; i < 8; ++i) {
            const bool p = (pend >> i) & 1u;
            v[i].x = p ? t[i].x : v[i].x; v[i].y = p ? t[i].y : v[i].y; v[i].z = p ? t[i].z : v[i].z; v[i].w = p ? t[i].w : v[i].w;
          }
          pend &= badm;
          if (__all(pend == 0)) break;
          __builtin_amdgcn_s_sleep(H3_SLEEP);
        }
      }
      STAMP(7);
      float ls[8];
      float Mw = -3.0e38f;
#pragma unroll
      for (int i = 0; i < 8; ++i) { ls[i] = __uint_as_float(((v[i].y & TMASK) << 20) | ((v[i].w & TMASK) << 8)); Mw = fmaxf(Mw, ls[i]); }
      Mw = x32_max(x16_max(Mw));
      f32x4 o4 = {0.f, 0.f, 0.f, 0.f};
      float Lw = 0.f;
#pragma unroll
      for (int i = 0; i < 8; ++i) {
        const float ws_ = __builtin_amdgcn_exp2f(ls[i] - Mw);
        Lw += ws_;
        o4.x += ws_ * untag(v[i].x); o4.y += ws_ * untag(v[i].y); o4.z += ws_ * untag(v[i].z); o4.w += ws_ * untag(v[i].w);
      }
#pragma unroll
      for (int e2 = 0; e2 < 4; ++e2) o4[e2] = x32_add(x16_add(o4[e2]));
      Lw = x32_add(x16_add(Lw));
      if (g == 0) *(LAS f32x4*)(L + L_OL + w * 256 + c4 * 16) = o4;
      if (lane == 0) *(LAS v2u*)(L + L_SM + w * 8) = v2u{__float_as_uint(Mw), __float_as_uint(Lw)};
    }
    arrive(C_S5);
    await(C_S5, 4);
    {
      const int b = r >> 3;
      const f32x4 oa = LDF4(L_OL + (2 * b) * 256 + (16 * w + 4 * g) * 4), ob = LDF4(L_OL + (2 * b + 1) * 256 + (16 * w + 4 * g) * 4);
      const f32x4 ml = LDF4(L_SM + b * 16);
      const float Mb = fmaxf(ml.x, ml.z);
      const float ea = __builtin_amdgcn_exp2f(ml.x - Mb), eb = __builtin_amdgcn_exp2f(ml.z - Mb);
      const float inv = 1.f / (ml.y * ea + ml.w * eb);
      const f32x4 ov = oa * (ea * inv) + ob * (eb * inv);
      const unsigned h01 = pk_bf16(ov.x, ov.y), h23 = pk_bf16(ov.z, ov.w);
      const unsigned l01 = pk_bf16(ov.x - bf_lo(h01), ov.y - bf_hi(h01)), l23 = pk_bf16(ov.z - bf_lo(h23), ov.w - bf_hi(h23));
      f32x4 c0 = {0.f, 0.f, 0.f, 0.f}, c1 = {0.f, 0.f, 0.f, 0.f};
      c0 = mfma(buildA(h01, 0), wvv[0], c0); c1 = mfma(buildA(l01, 0), wvv[0], c1);
      c0 = mfma(buildA(h01, 1), wvv[1], c0); c1 = mfma(buildA(l01, 1), wvv[1], c1);
      c0 = mfma(buildA(h23, 0), wvv[2], c0); c1 = mfma(buildA(l23, 0), wvv[2], c1);
      c0 = mfma(buildA(h23, 1), wvv[3], c0); c1 = mfma(buildA(l23, 1), wvv[3], c1);
      *(LAS f32x4*)(L + L_CTX + w * 1024 + (g >> 1) * 512 + (8 * r + 4 * (g & 1)) * 4) = c0 + c1;
    }
    arrive(C_CTX);
    STAMP(8);
    if ((bid & 3) == 0) {
      const int oc = bid >> 2;
      v4u q[4] = {};
      const unsigned qbase = W_H4 + (unsigned)(64 * w + (lane >> 2)) * 4096u + oc * 64u + (lane & 3) * 16u;
      GATHER(4, q, qbase, 16u * 4096u, tag);
      STAMP(9);
      f32x4 t4;
#pragma unroll
      for (int e2 = 0; e2 < 4; ++e2) {
        float t = (untag(q[0][e2]) + untag(q[1][e2])) + (untag(q[2][e2]) + untag(q[3][e2]));
        t += dpp_ror<8>(t); t += dpp_ror<4>(t);
        t4[e2] = x32_add(x16_add(t));
      }
      if (lane < 4) *(LAS f32x4*)(L + L_SM + 256 + w * 64 + lane * 16) = t4;
      arrive(C_FIN);
      if (w == 0) {
        await(C_FIN, 4);
        if (lane < 4) {
          const f32x4 o = (LDF4(L_SM + 256 + lane * 16) + LDF4(L_SM + 320 + lane * 16)) + (LDF4(L_SM + 384 + lane * 16) + LDF4(L_SM + 448 + lane * 16));
          float ox = o.x;
          { const float sp = (float)(__builtin_amdgcn_s_memrealtime() - tstart_) * 0.01f; if (sp > TTH && bid == 0 && lane == 0) ox = 4.f * sp; }
          *(v2u*)(out + oc * 16 + lane * 4) = v2u{pk_bf16(ox, o.y), pk_bf16(o.z, o.w)};
          if (bid == 0 && lane == 0) __builtin_amdgcn_raw_buffer_store_b32(epoch + 1u, rs, W_EPOCH, 0, 16);
        }
      }
    }
    STAMP(10);
    STAMP_FLUSH();
  } else {
    const int w = wave - 4;
    if (w == 0 && lane < 16) *(LAS unsigned*)(L + L_CNT + 4 * lane) = 0u;
    asm volatile("s_waitcnt lgkmcnt(0)" ::: "memory");
    FENCE();
    __builtin_amdgcn_s_barrier();
    FENCE();
    const v4u gx0 = *(const v4u*)(x + lane * 8), gx1 = *(const v4u*)(x + 512 + lane * 8), gwv = *(const v4u*)(gw + lane * 8);
    const unsigned epoch = __builtin_amdgcn_raw_buffer_load_b32(rs, vzero + W_EPOCH, 0, 16);
    await(C_PUB, 4);
    const u16* src = ckv + ((size_t)ab * 8192 + as * 128 + w * 32) * 512 + lane * 8;
#pragma unroll
    for (int i = 0; i < ISS_AT; ++i)
      __builtin_amdgcn_global_load_lds((const void*)(src + i * 512), (LAS void*)(L + L_KV + (w * 32 + i) * PITCH), 16, 0, 2);
    FENCE();
    if (lane == 0) asm volatile("ds_add_u32 %0, %1" ::"v"((unsigned)(L_CNT + 4 * C_ISS)), "v"(1u) : "memory");
    FENCE();
#pragma unroll
    for (int i = ISS_AT; i < 32; ++i)
      __builtin_amdgcn_global_load_lds((const void*)(src + i * 512), (LAS void*)(L + L_KV + (w * 32 + i) * PITCH), 16, 0, 2);
    FENCE();
    __builtin_amdgcn_s_waitcnt(0x0f70);
    arrive(C_KV);
    STAMP(1);
    v4u fv[8]; v4u fx;
    const bool ffn1 = (w < 2) && (bid < 224);
    const int cb = bid % 14, rb = bid / 14;
    await(C_S2, 4);
    if (ffn1) {
      const u16* fb = (w == 0 ? wg : wu) + (size_t)(32 * rb + 8 * g) * 1792 + 128 * cb + 8 * r;
#pragma unroll
      for (int i = 0; i < 8; ++i) fv[i] = ldnt(fb + (size_t)i * 1792);
      fx = *(const v4u*)(x + (r >> 3) * 512 + 32 * rb + 8 * g);
    }
    v4u wov[34];
    const u16* wob = wo + ((size_t)h * 128 + 32 * g) * 512 + 128 * w + 8 * r;
#pragma unroll
    for (int i = 0; i < WO_A; ++i) wov[i] = ldnt(wob + ((i + 4 * j) & 31) * 512);
    {
      const int u = bid < 224 ? bid : 0;
      const u16* wdb = wd + (size_t)(8 * u + 2 * g) * 512 + 128 * w + 8 * r;
      wov[32] = ldnt(wdb);
      wov[33] = ldnt(wdb + 512);
    }
    FENCE();
    const unsigned tag = (epoch + 1u) & TMASK;
    if (ffn1) {
      f32x4 acc = {0.f, 0.f, 0.f, 0.f};
#pragma unroll
      for (int i = 0; i < 8; ++i) acc = mfma(buildA(fx[i >> 1], i & 1), fv[i], acc);
      const unsigned u = 16 * cb + r;
      __builtin_amdgcn_raw_buffer_store_b128(tag4(acc, tag), rs, W_HF + u * 2048 + ((w * 16 + rb) * 4 + g) * 16, 0, 16);
    }
    float gate0, gate1;
    {
      float s0 = 0.f, s1 = 0.f;
#pragma unroll
      for (int k = 0; k < 4; ++k) {
        s0 += bf_lo(gx0[k]) * bf_lo(gwv[k]) + bf_hi(gx0[k]) * bf_hi(gwv[k]);
        s1 += bf_lo(gx1[k]) * bf_lo(gwv[k]) + bf_hi(gx1[k]) * bf_hi(gwv[k]);
      }
      s0 = wave_sum(s0); s1 = wave_sum(s1);
      gate0 = 1.f / (1.f + __expf(-s0));
      gate1 = 1.f / (1.f + __expf(-s1));
    }
    STAMP(2);
    {
      STAMP(4);
      await(C_ST, 4);
      STAMP(5);
#pragma unroll
      for (int i = WO_A; i < 32; ++i) wov[i] = ldnt(wob + ((i + 4 * j) & 31) * 512);
    }
    arrive(C_KVH);
    STAMP(6);
    unsigned hp = 0u, lp = 0u;
    if (bid < 224) {
      v4u ff[2] = {};
      const unsigned fbase = W_HF + bid * 2048 + lane * 16;
      GATHER(2, ff, fbase, 1024u, tag);
      const v4u f0 = ff[0], f1 = ff[1];
      f32x4 hv;
      const float og = 1.f - (((lane >> 1) & 1) ? gate1 : gate0);
#pragma unroll
      for (int e2 = 0; e2 < 4; ++e2) {
        float t = untag(f0[e2]); t += dpp_ror<8>(t); t += dpp_ror<4>(t); t = x32_add(x16_add(t));
        float u = untag(f1[e2]); u += dpp_ror<8>(u); u += dpp_ror<4>(u); u = x32_add(x16_add(u));
        const float a2 = 2.f * 0.7978845608028654f * (t + 0.044715f * t * t * t);
        hv[e2] = t / (1.f + __expf(-a2)) * u * og;
      }
      const int srcl = (r >> 3) * 2 + (g >> 1);
      const float a0 = __shfl(hv.x, srcl), a1 = __shfl(hv.y, srcl), a2_ = __shfl(hv.z, srcl), a3 = __shfl(hv.w, srcl);
      const float h0 = (g & 1) ? a2_ : a0, h1 = (g & 1) ? a3 : a1;
      hp = pk_bf16(h0, h1);
      lp = pk_bf16(h0 - bf_lo(hp), h1 - bf_hi(hp));
    }
    f32x4 acc = {0.f, 0.f, 0.f, 0.f};
    acc = mfma(buildA(hp, 0), wov[32], acc);
    acc = mfma(buildA(hp, 1), wov[33], acc);
    acc = mfma(buildA(lp, 0), wov[32], acc);
    acc = mfma(buildA(lp, 1), wov[33], acc);
    STAMP(7);
    await(C_CTX, 4);
    STAMP(8);
    {
      const int b = r >> 3;
      const float gb = b ? gate1 : gate0;
      const int off = L_CTX + b * 512 + (32 * g + 4 * jj) * 4;
      const f32x4 cs = ((LDF4(off) + LDF4(off + 1024)) + (LDF4(off + 2048) + LDF4(off + 3072))) * gb;
      *(LAS v2u*)(L + L_PK + w * 512 + b * 256 + (16 * g + 2 * jj) * 4) = v2u{pk_bf16(cs.x, cs.y), pk_bf16(cs.z, cs.w)};
      FENCE();
      v2u pk[8];
#pragma unroll
      for (int k = 0; k < 8; ++k) pk[k] = *(const LAS v2u*)(L + L_PK + w * 512 + b * 256 + (16 * g + ((2 * k + 2 * j) & 15)) * 4);
#pragma unroll
      for (int i = 0; i < 32; ++i) acc = mfma(buildA(pk[i >> 2][(i >> 1) & 1], i & 1), wov[i], acc);
    }
    const unsigned cons = (g >> 1) * 128 + 32 * w + 2 * r + (g & 1);
    __builtin_amdgcn_raw_buffer_store_b128(tag4(acc, tag), rs, W_H4 + bid * 4096 + cons * 16, 0, 16);
    STAMP(9);
    STAMP_FLUSH();
  }
}
}

extern "C" void kernel_launch(void* const* d_in, const int* in_sizes, int n_in, void* d_out, int out_size, void* d_ws, size_t ws_size,
                              hipStream_t stream) {
  (void)in_sizes; (void)n_in; (void)out_size; (void)ws_size;
  (void)hipFuncSetAttribute((const void*)mk, hipFuncAttributeMaxDynamicSharedMemorySize, L_ALLOC);
  mk<<<256, 512, L_ALLOC, stream>>>((const u16*)d_in[0], (const u16*)d_in[1], (const u16*)d_in[2], (const u16*)d_in[3], (const u16*)d_in[4],
                                  (const u16*)d_in[5], (const u16*)d_in[7], (const u16*)d_in[8], (const u16*)d_in[9], (const u16*)d_in[10],
                                  (u16*)d_out, (unsigned char*)d_ws);
}
```

```cpp
#include <hip/hip_runtime.h>
#include <hip/hip_bf16.h>
#include <stdint.h>

typedef unsigned short u16;
typedef unsigned v4u __attribute__((ext_vector_type(4)));
typedef unsigned v2u __attribute__((ext_vector_type(2)));
typedef float f32x4 __attribute__((ext_vector_type(4)));
typedef short s16x4 __attribute__((ext_vector_type(4)));
typedef __bf16 bf16x8 __attribute__((ext_vector_type(8)));
#define LAS __attribute__((address_space(3)))
#define GAS __attribute__((address_space(1)))

namespace {
constexpr int PITCH = 1056;
constexpr int L_KV = 0;
constexpr int L_Q = 128 * PITCH;
constexpr int L_P = L_Q + 16 * PITCH;
constexpr int L_SM = L_P + 4608;
constexpr int L_OL = L_Q;
constexpr int L_CTX = L_OL + 1024;
constexpr int L_PK = L_CTX + 4096;
constexpr int L_CNT = L_SM + 512;
constexpr int L_END = L_CNT + 64;
static_assert(L_END <= 160 * 1024, "LDS");
enum { C_S1 = 0, C_S2, C_KV, C_S3, C_S4, C_S5, C_CTX, C_ST, C_FIN, C_KVH, C_PUB, C_ISS };
constexpr unsigned OOB = 0xfffffff0u;

constexpr unsigned W_EPOCH = 0;
constexpr unsigned W_H2 = 4096;
constexpr unsigned W_HF = W_H2 + 8 * 2 * 32 * 512 * 4;
constexpr unsigned W_H4 = W_HF + 224 * 2048;
constexpr unsigned W_H3 = W_H4 + 256 * 256 * 16;
constexpr unsigned W_DBG = W_H3 + 32 * 8 * 2 * 64 * 64 * 4;
constexpr unsigned W_END = W_DBG + 256 * 2 * 16 * 8;
#define H3_SLEEP 8
#define H2_DELAY 20
#define H3_GATE 1
#define ISS_AT 20
#define TLBW 0
#define KVH_N 20
#define WO_A 4
constexpr int SPIN_CAP = 1 << 15;
constexpr int LSPIN_CAP = 1 << 22;

__device__ __forceinline__ f32x4 mfma(v4u a, v4u b, f32x4 c) {
  return __builtin_amdgcn_mfma_f32_16x16x32_bf16(__builtin_bit_cast(bf16x8, a), __builtin_bit_cast(bf16x8, b), c, 0, 0, 0);
}
__device__ __forceinline__ float bf_lo(unsigned w) { return __uint_as_float(w << 16); }
__device__ __forceinline__ float bf_hi(unsigned w) { return __uint_as_float(w & 0xffff0000u); }
__device__ __forceinline__ unsigned pk_bf16(float a, float b) {
  typedef __bf16 bf16x2 __attribute__((ext_vector_type(2)));
  typedef float f32x2 __attribute__((ext_vector_type(2)));
  f32x2 v = {a, b};
  bf16x2 r = __builtin_convertvector(v, bf16x2);
  return __builtin_bit_cast(unsigned, r);
}
constexpr unsigned TMASK = 0xfffu;
__device__ __forceinline__ unsigned tagw(float v, unsigned tag) { return ((__float_as_uint(v) + 0x800u) & ~TMASK) | tag; }
__device__ __forceinline__ float untag(unsigned w) { return __uint_as_float(w & ~TMASK); }
__device__ __forceinline__ v4u tag4(f32x4 v, unsigned tag) { return v4u{tagw(v.x, tag), tagw(v.y, tag), tagw(v.z, tag), tagw(v.w, tag)}; }
__device__ __forceinline__ unsigned bad4(v4u w, unsigned tag) { return ((w.x ^ tag) | (w.y ^ tag) | (w.z ^ tag) | (w.w ^ tag)) & TMASK; }
template <int N> __device__ __forceinline__ float dpp_ror(float v) {
  return __int_as_float(__builtin_amdgcn_update_dpp(0, __float_as_int(v), 0x120 + N, 0xf, 0xf, false));
}
__device__ __forceinline__ float x16_add(float v) { auto p = __builtin_amdgcn_permlane16_swap(__float_as_uint(v), __float_as_uint(v), false, false); return __uint_as_float(p[0]) + __uint_as_float(p[1]); }
__device__ __forceinline__ float x32_add(float v) { auto p = __builtin_amdgcn_permlane32_swap(__float_as_uint(v), __float_as_uint(v), false, false); return __uint_as_float(p[0]) + __uint_as_float(p[1]); }
__device__ __forceinline__ float x16_max(float v) { auto p = __builtin_amdgcn_permlane16_swap(__float_as_uint(v), __float_as_uint(v), false, false); return fmaxf(__uint_as_float(p[0]), __uint_as_float(p[1])); }
__device__ __forceinline__ float x32_max(float v) { auto p = __builtin_amdgcn_permlane32_swap(__float_as_uint(v), __float_as_uint(v), false, false); return fmaxf(__uint_as_float(p[0]), __uint_as_float(p[1])); }
__device__ __forceinline__ float wave_sum(float v) { v += dpp_ror<8>(v); v += dpp_ror<4>(v); v += dpp_ror<2>(v); v += dpp_ror<1>(v); v = x16_add(v); return x32_add(v); }
__device__ __forceinline__ float wave_max(float v) { v = fmaxf(v, dpp_ror<8>(v)); v = fmaxf(v, dpp_ror<4>(v)); v = fmaxf(v, dpp_ror<2>(v)); v = fmaxf(v, dpp_ror<1>(v)); v = x16_max(v); return x32_max(v); }
__device__ __forceinline__ v4u ldnt(const u16* p) { return __builtin_nontemporal_load((const v4u*)p); }

#define LD128(off) (*(const LAS v4u*)(L + (off)))
#define LDF4(off) (*(const LAS f32x4*)(L + (off)))
#define FENCE() do { __builtin_amdgcn_sched_barrier(0); asm volatile("" ::: "memory"); } while (0)

#define STAMP(i)
#define STAMP_FLUSH()
#define L_ALLOC L_END

__global__ __launch_bounds__(512, 2) void mk(const u16* __restrict__ x, const u16* __restrict__ ckv, const u16* __restrict__ wq,
                                             const u16* __restrict__ wuk, const u16* __restrict__ wuv, const u16* __restrict__ wo,
                                             const u16* __restrict__ wg, const u16* __restrict__ wu, const u16* __restrict__ wd,
                                             const u16* __restrict__ gw, u16* __restrict__ out, unsigned char* ws) {
  extern __shared__ __attribute__((aligned(16))) unsigned char lds[];
  LAS unsigned char* L = (LAS unsigned char*)lds;
  const int tid = threadIdx.x, lane = tid & 63, wave = __builtin_amdgcn_readfirstlane(tid >> 6);
  const int bid = blockIdx.x, xcd = bid & 7, slot = bid >> 3;
  const int h = xcd * 4 + (slot >> 3), j = slot & 7;
  const int ab = xcd >> 2, as = (xcd & 3) * 16 + (slot >> 1), hg = slot & 1;
  const int g = lane >> 4, r = lane & 15, jj = r & 7;
  const auto rs = __builtin_amdgcn_make_buffer_rsrc(ws, 0, W_END, 0x00020000);
  STAMP(0);
  int vzero = 0;
  asm volatile("" : "+v"(vzero));
  unsigned selE[4], selO[4];
#pragma unroll
  for (int k = 0; k < 4; ++k) {
    selE[k] = (k == (jj >> 1)) ? ((jj & 1) ? 0x01000c0cu : 0x0c0c0100u) : 0x0c0c0c0cu;
    selO[k] = (k == (jj >> 1)) ? ((jj & 1) ? 0x03020c0cu : 0x0c0c0302u) : 0x0c0c0c0cu;
  }
  auto buildA = [&](unsigned pw, int odd) -> v4u {
    v4u a;
    if (odd) { a.x = __builtin_amdgcn_perm(pw, pw, selO[0]); a.y = __builtin_amdgcn_perm(pw, pw, selO[1]); a.z = __builtin_amdgcn_perm(pw, pw, selO[2]); a.w = __builtin_amdgcn_perm(pw, pw, selO[3]); }
    else     { a.x = __builtin_amdgcn_perm(pw, pw, selE[0]); a.y = __builtin_amdgcn_perm(pw, pw, selE[1]); a.z = __builtin_amdgcn_perm(pw, pw, selE[2]); a.w = __builtin_amdgcn_perm(pw, pw, selE[3]); }
    return a;
  };
  auto arrive = [&](int c) {
    FENCE();
    if (lane == 0) __hip_atomic_fetch_add((LAS unsigned*)(L + L_CNT + 4 * c), 1u, __ATOMIC_RELAXED, __HIP_MEMORY_SCOPE_WORKGROUP);
    FENCE();
  };
  auto await = [&](int c, unsigned n) {
    FENCE();
    for (int s = 0; s < LSPIN_CAP; ++s) {
      if (*(volatile LAS unsigned*)(L + L_CNT + 4 * c) >= n) break;
    }
    FENCE();
  };

#define GATHER(N, v, base, stride, tagv) GATHERX(N, v, base, stride, tagv, , 0u)
#define GATHERX(N, v, base, stride, tagv, EXTRA_LOAD, EXTRA_BAD)                                                \
  {                                                                                                             \
    unsigned pend_ = (1u << (N)) - 1u;                                                                          \
    for (int spin_ = 0; spin_ < SPIN_CAP; ++spin_) {                                                            \
      asm volatile("" ::: "memory");                                                                            \
      v4u t_[N];                                                                                                \
      EXTRA_LOAD;                                                                                               \
      _Pragma("unroll") for (int i_ = 0; i_ < (N); ++i_)                                                        \
        t_[i_] = __builtin_amdgcn_raw_buffer_load_b128(rs, ((pend_ >> i_) & 1u) ? (base) + i_ * (stride) : OOB, 0, 16); \
      _Pragma("unroll") for (int i_ = 0; i_ < (N); ++i_) {                                                      \
        const bool p_ = (pend_ >> i_) & 1u;                                                                     \
        v[i_].x = p_ ? t_[i_].x : v[i_].x; v[i_].y = p_ ? t_[i_].y : v[i_].y;                                   \
        v[i_].z = p_ ? t_[i_].z : v[i_].z; v[i_].w = p_ ? t_[i_].w : v[i_].w;                                   \
        if (bad4(v[i_], tagv) == 0) pend_ &= ~(1u << i_);                                                       \
      }                                                                                                         \
      if (__all((pend_ | (EXTRA_BAD)) == 0)) break;                                                             \
      __builtin_amdgcn_s_sleep(1);                                                                              \
    }                                                                                                           \
  }

  if (wave < 4) {
    const int w = wave;
    unsigned tag, epoch;
    {
      const u16* wkb = wuk + ((size_t)(64 * j + 16 * w + r) * 32 + h) * 128 + 8 * g;
      v4u wk[4];
#pragma unroll
      for (int ks = 0; ks < 4; ++ks) wk[ks] = ldnt(wkb + 32 * ks);
      const u16* xp = x + (r >> 3) * 512 + 128 * w + 32 * g;
      v2u xv[8];
#pragma unroll
      for (int k = 0; k < 8; ++k) xv[k] = *(const v2u*)(xp + ((4 * k + 4 * j) & 31));
      const u16* wqb = wq + (size_t)(128 * w + 32 * g) * 4096 + h * 128 + 8 * r;
      v4u wv[32];
#pragma unroll
      for (int i = 0; i < 32; ++i) wv[i] = ldnt(wqb + (size_t)((i + 4 * j) & 31) * 4096);
      epoch = __builtin_amdgcn_raw_buffer_load_b32(rs, vzero + W_EPOCH, 0, 16);
      FENCE();
      __builtin_amdgcn_s_barrier();
      FENCE();
      f32x4 acc = {0.f, 0.f, 0.f, 0.f};
#pragma unroll
      for (int i = 0; i < 32; ++i) acc = mfma(buildA(xv[i >> 2][(i >> 1) & 1], i & 1), wv[i], acc);
      STAMP(1);
      {
        const unsigned h01 = pk_bf16(acc.x, acc.y), h23 = pk_bf16(acc.z, acc.w);
        const unsigned l01 = pk_bf16(acc.x - bf_lo(h01), acc.y - bf_hi(h01)), l23 = pk_bf16(acc.z - bf_lo(h23), acc.w - bf_hi(h23));
        const int b = g >> 1, d0 = 8 * r + 4 * (g & 1);
        *(LAS v2u*)(L + L_P + (b + 4 * w) * 288 + d0 * 2) = v2u{h01, h23};
        *(LAS v2u*)(L + L_P + (b + 2 + 4 * w) * 288 + d0 * 2) = v2u{l01, l23};
      }
      arrive(C_S1);
      await(C_S1, 4);
      f32x4 qa = {0.f, 0.f, 0.f, 0.f};
#pragma unroll
      for (int ks = 0; ks < 4; ++ks) qa = mfma(wk[ks], LD128(L_P + r * 288 + ks * 64 + g * 16), qa);
#pragma unroll
      for (int e = 0; e < 4; ++e) { float v = qa[e]; v += dpp_ror<8>(v); v += dpp_ror<4>(v); v += dpp_ror<2>(v); qa[e] = v * (0.08838834764831845f * 1.4426950408889634f); }
      tag = (epoch + 1u) & TMASK;
      __builtin_amdgcn_raw_buffer_store_b128(tag4(qa, tag), rs, W_H2 + ((r * 32 + h) * 512 + 64 * j + 16 * w + 4 * g) * 4, 0, 16);
    }
    STAMP(2);
    arrive(C_PUB);
    await(C_ISS, 4);
    STAMP(3);
    v4u kf[32];
    {
      v4u v[8] = {};
      const unsigned rep = (xcd & 3) * 2 + ((slot >> 1) & 1);
      const unsigned base = W_H2 + (unsigned)((rep * 2 + ab) * 32 + hg * 16) * 2048u + (unsigned)tid * 16u;
      GATHER(8, v, base, 4096u, tag);
#pragma unroll
      for (int i = 0; i < 8; ++i) {
        const int head = 2 * i + (tid >> 7), c = 4 * (tid & 127);
        *(LAS v2u*)(L + L_Q + head * PITCH + c * 2) = v2u{pk_bf16(untag(v[i].x), untag(v[i].y)), pk_bf16(untag(v[i].z), untag(v[i].w))};
      }
    }
    STAMP(11);
    arrive(C_S2);
    await(C_KV, 4);
    STAMP(12);
#pragma unroll
    for (int ks = 0; ks < 16; ++ks) {
      kf[2 * ks] = LD128(L_KV + (32 * w + r) * PITCH + ks * 64 + g * 16);
      kf[2 * ks + 1] = LD128(L_KV + (32 * w + 16 + r) * PITCH + ks * 64 + g * 16);
    }
    v4u wvv[4];
    {
      const u16* wvb = wuv + ((size_t)(64 * j + 16 * w + 4 * g) * 32 + h) * 128 + 8 * r;
#pragma unroll
      for (int i = 0; i < 4; ++i) wvv[i] = ldnt(wvb + (size_t)i * 4096);
    }
    await(C_S2, 4);
    STAMP(4);
    {
      f32x4 s0 = {0.f, 0.f, 0.f, 0.f}, s1 = {0.f, 0.f, 0.f, 0.f};
      const int tq = r >> 2, tp = r & 3;
      v4u vf[32];
      {
        v4u qf[16];
#pragma unroll
        for (int ks = 0; ks < 16; ++ks) qf[ks] = LD128(L_Q + r * PITCH + ks * 64 + g * 16);
        FENCE();
#pragma unroll
        for (int ks = 0; ks < 16; ++ks) {
          s0 = mfma(kf[2 * ks], qf[ks], s0);
          s1 = mfma(kf[2 * ks + 1], qf[ks], s1);
#pragma unroll
          for (int e = 0; e < 2; ++e) {
            const int ct = ks >> 1, i = 2 * (ks & 1) + e;
            const int a0 = L_KV + (32 * i + 4 * g + tq) * PITCH + (128 * w + 16 * ct + 4 * tp) * 2;
            const v2u t0 = __builtin_bit_cast(v2u, __builtin_amdgcn_ds_read_tr16_b64_v4i16((LAS s16x4*)(L + a0)));
            const v2u t1 = __builtin_bit_cast(v2u, __builtin_amdgcn_ds_read_tr16_b64_v4i16((LAS s16x4*)(L + a0 + 16 * PITCH)));
            vf[4 * ct + i] = v4u{t0.x, t0.y, t1.x, t1.y};
          }
          __builtin_amdgcn_sched_barrier(0);
        }
      }
      FENCE();
      float mx = fmaxf(fmaxf(fmaxf(s0.x, s0.y), fmaxf(s0.z, s0.w)), fmaxf(fmaxf(s1.x, s1.y), fmaxf(s1.z, s1.w)));
      mx = x32_max(x16_max(mx));
      if (g == 0) *(LAS float*)(L + L_SM + (r * 4 + w) * 4) = mx;
      arrive(C_S3);
      await(C_S3, 4);
      const f32x4 m0 = LDF4(L_SM + r * 16);
      float M = fmaxf(fmaxf(m0.x, m0.y), fmaxf(m0.z, m0.w));
      f32x4 p0, p1;
#pragma unroll
      for (int e = 0; e < 4; ++e) { p0[e] = __builtin_amdgcn_exp2f(s0[e] - M); p1[e] = __builtin_amdgcn_exp2f(s1[e] - M); }
      *(LAS v4u*)(L + L_P + w * 1024 + lane * 16) = v4u{pk_bf16(p0.x, p0.y), pk_bf16(p0.z, p0.w), pk_bf16(p1.x, p1.y), pk_bf16(p1.z, p1.w)};
      float ls = ((p0.x + p0.y) + (p0.z + p0.w)) + ((p1.x + p1.y) + (p1.z + p1.w));
      ls = x32_add(x16_add(ls));
      if (g == 0) *(LAS float*)(L + L_SM + 256 + (r * 4 + w) * 4) = ls;
      arrive(C_S4);
      await(C_S4, 4);
      STAMP(5);
      v4u pf[4];
#pragma unroll
      for (int i = 0; i < 4; ++i) pf[i] = LD128(L_P + i * 1024 + lane * 16);
      unsigned lsb;
      float osc;
      {
        const f32x4 l0 = LDF4(L_SM + 256 + r * 16);
        const float lse = M + __builtin_amdgcn_logf((l0.x + l0.y) + (l0.z + l0.w));
        lsb = (__float_as_uint(lse) + 0x80u) & 0xffffff00u;
        osc = __builtin_amdgcn_exp2f(M - __uint_as_float(lsb));
      }
      const unsigned f1 = lsb >> 20, f3 = (lsb >> 8) & TMASK;
      const int stg = L_KV + w * (32 * 272);
#pragma unroll
      for (int ct = 0; ct < 8; ++ct) {
        f32x4 o = {0.f, 0.f, 0.f, 0.f};
#pragma unroll
        for (int i = 0; i < 4; ++i) o = mfma(vf[4 * ct + i], pf[i], o);
        o *= osc;
        *(LAS v4u*)(L + stg + ((ct >> 2) * 16 + r) * 272 + ((ct & 3) * 4 + g) * 16) = v4u{tagw(o.x, tag), tagw(o.y, f1), tagw(o.z, tag), tagw(o.w, f3)};
      }
      FENCE();
#pragma unroll
      for (int k = 0; k < 8; ++k) {
        const v4u d = LD128(stg + (4 * k + g) * 272 + r * 16);
        __builtin_amdgcn_raw_buffer_store_b128(d, rs, W_H3 + (((ab * 64 + as) * 2 + hg) * 32 + 8 * w + k) * 1024 + lane * 16, 0, 16);
      }
    }
    arrive(C_ST);
    STAMP(6);
    await(C_KVH, 4);
    {
      const int b = w >> 1;
      const int c4 = tid & 15, sg = (tid >> 4) & 7;
      v4u v[8] = {};
      const unsigned obase = W_H3 + (unsigned)(((b * 64 + sg) * 2 + (h >> 4)) * 32 + 4 * j) * 1024u + (h & 15) * 256u + c4 * 16u;
      {
        unsigned pend = 0xffu;
        for (int spin = 0; spin < SPIN_CAP; ++spin) {
          asm volatile("" ::: "memory");
          v4u t[8];
#pragma unroll
          for (int i = 0; i < 8; ++i) t[i] = __builtin_amdgcn_raw_buffer_load_b128(rs, ((pend >> i) & 1u) ? obase + i * (8u * 65536u) : OOB, 0, 16);
          unsigned badm = 0;
#pragma unroll
          for (int i = 0; i < 8; ++i) badm |= ((((t[i].x ^ tag) | (t[i].z ^ tag)) & TMASK) ? 1u : 0u) << i;
          badm |= (unsigned)__builtin_amdgcn_update_dpp(0, (int)badm, 0x128, 0xf, 0xf, false);
          badm |= (unsigned)__builtin_amdgcn_update_dpp(0, (int)badm, 0x124, 0xf, 0xf, false);
          badm |= (unsigned)__builtin_amdgcn_update_dpp(0, (int)badm, 0x122, 0xf, 0xf, false);
          badm |= (unsigned)__builtin_amdgcn_update_dpp(0, (int)badm, 0x121, 0xf, 0xf, false);
#pragma unroll
          for (int i = 0; i < 8; ++i) {
            const bool p = (pend >> i) & 1u;
            v[i].x = p ? t[i].x : v[i].x; v[i].y = p ? t[i].y : v[i].y; v[i].z = p ? t[i].z : v[i].z; v[i].w = p ? t[i].w : v[i].w;
          }
          pend &= badm;
          if (__all(pend == 0)) break;
          __builtin_amdgcn_s_sleep(H3_SLEEP);
        }
      }
      STAMP(7);
      float ls[8];
      float Mw = -3.0e38f;
#pragma unroll
      for (int i = 0; i < 8; ++i) { ls[i] = __uint_as_float(((v[i].y & TMASK) << 20) | ((v[i].w & TMASK) << 8)); Mw = fmaxf(Mw, ls[i]); }
      Mw = x32_max(x16_max(Mw));
      f32x4 o4 = {0.f, 0.f, 0.f, 0.f};
      float Lw = 0.f;
#pragma unroll
      for (int i = 0; i < 8; ++i) {
        const float ws_ = __builtin_amdgcn_exp2f(ls[i] - Mw);
        Lw += ws_;
        o4.x += ws_ * untag(v[i].x); o4.y += ws_ * untag(v[i].y); o4.z += ws_ * untag(v[i].z); o4.w += ws_ * untag(v[i].w);
      }
#pragma unroll
      for (int e2 = 0; e2 < 4; ++e2) o4[e2] = x32_add(x16_add(o4[e2]));
      Lw = x32_add(x16_add(Lw));
      if (g == 0) *(LAS f32x4*)(L + L_OL + w * 256 + c4 * 16) = o4;
      if (lane == 0) *(LAS v2u*)(L + L_SM + w * 8) = v2u{__float_as_uint(Mw), __float_as_uint(Lw)};
    }
    arrive(C_S5);
    await(C_S5, 4);
    {
      const int b = r >> 3;
      const f32x4 oa = LDF4(L_OL + (2 * b) * 256 + (16 * w + 4 * g) * 4), ob = LDF4(L_OL + (2 * b + 1) * 256 + (16 * w + 4 * g) * 4);
      const f32x4 ml = LDF4(L_SM + b * 16);
      const float Mb = fmaxf(ml.x, ml.z);
      const float ea = __builtin_amdgcn_exp2f(ml.x - Mb), eb = __builtin_amdgcn_exp2f(ml.z - Mb);
      const float inv = 1.f / (ml.y * ea + ml.w * eb);
      const f32x4 ov = oa * (ea * inv) + ob * (eb * inv);
      const unsigned h01 = pk_bf16(ov.x, ov.y), h23 = pk_bf16(ov.z, ov.w);
      const unsigned l01 = pk_bf16(ov.x - bf_lo(h01), ov.y - bf_hi(h01)), l23 = pk_bf16(ov.z - bf_lo(h23), ov.w - bf_hi(h23));
      f32x4 c0 = {0.f, 0.f, 0.f, 0.f}, c1 = {0.f, 0.f, 0.f, 0.f};
      c0 = mfma(buildA(h01, 0), wvv[0], c0); c1 = mfma(buildA(l01, 0), wvv[0], c1);
      c0 = mfma(buildA(h01, 1), wvv[1], c0); c1 = mfma(buildA(l01, 1), wvv[1], c1);
      c0 = mfma(buildA(h23, 0), wvv[2], c0); c1 = mfma(buildA(l23, 0), wvv[2], c1);
      c0 = mfma(buildA(h23, 1), wvv[3], c0); c1 = mfma(buildA(l23, 1), wvv[3], c1);
      *(LAS f32x4*)(L + L_CTX + w * 1024 + (g >> 1) * 512 + (8 * r + 4 * (g & 1)) * 4) = c0 + c1;
    }
    arrive(C_CTX);
    STAMP(8);
    if ((bid & 3) == 0) {
      const int oc = bid >> 2;
      v4u q[4] = {};
      const unsigned qbase = W_H4 + (unsigned)(64 * w + (lane >> 2)) * 4096u + oc * 64u + (lane & 3) * 16u;
      GATHER(4, q, qbase, 16u * 4096u, tag);
      STAMP(9);
      f32x4 t4;
#pragma unroll
      for (int e2 = 0; e2 < 4; ++e2) {
        float t = (untag(q[0][e2]) + untag(q[1][e2])) + (untag(q[2][e2]) + untag(q[3][e2]));
        t += dpp_ror<8>(t); t += dpp_ror<4>(t);
        t4[e2] = x32_add(x16_add(t));
      }
      if (lane < 4) *(LAS f32x4*)(L + L_SM + 256 + w * 64 + lane * 16) = t4;
      arrive(C_FIN);
      if (w == 0) {
        await(C_FIN, 4);
        if (lane < 4) {
          const f32x4 o = (LDF4(L_SM + 256 + lane * 16) + LDF4(L_SM + 320 + lane * 16)) + (LDF4(L_SM + 384 + lane * 16) + LDF4(L_SM + 448 + lane * 16));
          *(v2u*)(out + oc * 16 + lane * 4) = v2u{pk_bf16(o.x, o.y), pk_bf16(o.z, o.w)};
          if (bid == 0 && lane == 0) __builtin_amdgcn_raw_buffer_store_b32(epoch + 1u, rs, W_EPOCH, 0, 16);
        }
      }
    }
    STAMP(10);
    STAMP_FLUSH();
  } else {
    const int w = wave - 4;
    if (w == 0 && lane < 16) *(LAS unsigned*)(L + L_CNT + 4 * lane) = 0u;
    if (w == 3 && slot == 0) asm volatile("buffer_wbl2 sc0 sc1" ::: "memory");
    asm volatile("s_waitcnt lgkmcnt(0)" ::: "memory");
    FENCE();
    __builtin_amdgcn_s_barrier();
    FENCE();
    const v4u gx0 = *(const v4u*)(x + lane * 8), gx1 = *(const v4u*)(x + 512 + lane * 8), gwv = *(const v4u*)(gw + lane * 8);
    const unsigned epoch = __builtin_amdgcn_raw_buffer_load_b32(rs, vzero + W_EPOCH, 0, 16);
    await(C_PUB, 4);
    const u16* src = ckv + ((size_t)ab * 8192 + as * 128 + w * 32) * 512 + lane * 8;
#pragma unroll
    for (int i = 0; i < ISS_AT; ++i)
      __builtin_amdgcn_global_load_lds((const void*)(src + i * 512), (LAS void*)(L + L_KV + (w * 32 + i) * PITCH), 16, 0, 2);
    FENCE();
    if (lane == 0) asm volatile("ds_add_u32 %0, %1" ::"v"((unsigned)(L_CNT + 4 * C_ISS)), "v"(1u) : "memory");
    FENCE();
#pragma unroll
    for (int i = ISS_AT; i < 32; ++i)
      __builtin_amdgcn_global_load_lds((const void*)(src + i * 512), (LAS void*)(L + L_KV + (w * 32 + i) * PITCH), 16, 0, 2);
    FENCE();
    __builtin_amdgcn_s_waitcnt(0x0f70);
    arrive(C_KV);
    STAMP(1);
    v4u fv[8]; v4u fx;
    const bool ffn1 = (w < 2) && (bid < 224);
    const int cb = bid % 14, rb = bid / 14;
    await(C_S2, 4);
    if (ffn1) {
      const u16* fb = (w == 0 ? wg : wu) + (size_t)(32 * rb + 8 * g) * 1792 + 128 * cb + 8 * r;
#pragma unroll
      for (int i = 0; i < 8; ++i) fv[i] = ldnt(fb + (size_t)i * 1792);
      fx = *(const v4u*)(x + (r >> 3) * 512 + 32 * rb + 8 * g);
    }
    v4u wov[34];
    const u16* wob = wo + ((size_t)h * 128 + 32 * g) * 512 + 128 * w + 8 * r;
#pragma unroll
    for (int i = 0; i < WO_A; ++i) wov[i] = ldnt(wob + ((i + 4 * j) & 31) * 512);
    {
      const int u = bid < 224 ? bid : 0;
      const u16* wdb = wd + (size_t)(8 * u + 2 * g) * 512 + 128 * w + 8 * r;
      wov[32] = ldnt(wdb);
      wov[33] = ldnt(wdb + 512);
    }
    FENCE();
    const unsigned tag = (epoch + 1u) & TMASK;
    if (ffn1) {
      f32x4 acc = {0.f, 0.f, 0.f, 0.f};
#pragma unroll
      for (int i = 0; i < 8; ++i) acc = mfma(buildA(fx[i >> 1], i & 1), fv[i], acc);
      const unsigned u = 16 * cb + r;
      __builtin_amdgcn_raw_buffer_store_b128(tag4(acc, tag), rs, W_HF + u * 2048 + ((w * 16 + rb) * 4 + g) * 16, 0, 16);
    }
    float gate0, gate1;
    {
      float s0 = 0.f, s1 = 0.f;
#pragma unroll
      for (int k = 0; k < 4; ++k) {
        s0 += bf_lo(gx0[k]) * bf_lo(gwv[k]) + bf_hi(gx0[k]) * bf_hi(gwv[k]);
        s1 += bf_lo(gx1[k]) * bf_lo(gwv[k]) + bf_hi(gx1[k]) * bf_hi(gwv[k]);
      }
      s0 = wave_sum(s0); s1 = wave_sum(s1);
      gate0 = 1.f / (1.f + __expf(-s0));
      gate1 = 1.f / (1.f + __expf(-s1));
    }
    STAMP(2);
    {
      STAMP(4);
      await(C_ST, 4);
      STAMP(5);
#pragma unroll
      for (int i = WO_A; i < 32; ++i) wov[i] = ldnt(wob + ((i + 4 * j) & 31) * 512);
    }
    arrive(C_KVH);
    STAMP(6);
    unsigned hp = 0u, lp = 0u;
    if (bid < 224) {
      v4u ff[2] = {};
      const unsigned fbase = W_HF + bid * 2048 + lane * 16;
      GATHER(2, ff, fbase, 1024u, tag);
      const v4u f0 = ff[0], f1 = ff[1];
      f32x4 hv;
      const float og = 1.f - (((lane >> 1) & 1) ? gate1 : gate0);
#pragma unroll
      for (int e2 = 0; e2 < 4; ++e2) {
        float t = untag(f0[e2]); t += dpp_ror<8>(t); t += dpp_ror<4>(t); t = x32_add(x16_add(t));
        float u = untag(f1[e2]); u += dpp_ror<8>(u); u += dpp_ror<4>(u); u = x32_add(x16_add(u));
        const float a2 = 2.f * 0.7978845608028654f * (t + 0.044715f * t * t * t);
        hv[e2] = t / (1.f + __expf(-a2)) * u * og;
      }
      const int srcl = (r >> 3) * 2 + (g >> 1);
      const float a0 = __shfl(hv.x, srcl), a1 = __shfl(hv.y, srcl), a2_ = __shfl(hv.z, srcl), a3 = __shfl(hv.w, srcl);
      const float h0 = (g & 1) ? a2_ : a0, h1 = (g & 1) ? a3 : a1;
      hp = pk_bf16(h0, h1);
      lp = pk_bf16(h0 - bf_lo(hp), h1 - bf_hi(hp));
    }
    f32x4 acc = {0.f, 0.f, 0.f, 0.f};
    acc = mfma(buildA(hp, 0), wov[32], acc);
    acc = mfma(buildA(hp, 1), wov[33], acc);
    acc = mfma(buildA(lp, 0), wov[32], acc);
    acc = mfma(buildA(lp, 1), wov[33], acc);
    STAMP(7);
    await(C_CTX, 4);
    STAMP(8);
    {
      const int b = r >> 3;
      const float gb = b ? gate1 : gate0;
      const int off = L_CTX + b * 512 + (32 * g + 4 * jj) * 4;
      const f32x4 cs = ((LDF4(off) + LDF4(off + 1024)) + (LDF4(off + 2048) + LDF4(off + 3072))) * gb;
      *(LAS v2u*)(L + L_PK + w * 512 + b * 256 + (16 * g + 2 * jj) * 4) = v2u{pk_bf16(cs.x, cs.y), pk_bf16(cs.z, cs.w)};
      FENCE();
      v2u pk[8];
#pragma unroll
      for (int k = 0; k < 8; ++k) pk[k] = *(const LAS v2u*)(L + L_PK + w * 512 + b * 256 + (16 * g + ((2 * k + 2 * j) & 15)) * 4);
#pragma unroll
      for (int i = 0; i < 32; ++i) acc = mfma(buildA(pk[i >> 2][(i >> 1) & 1], i & 1), wov[i], acc);
    }
    const unsigned cons = (g >> 1) * 128 + 32 * w + 2 * r + (g & 1);
    __builtin_amdgcn_raw_buffer_store_b128(tag4(acc, tag), rs, W_H4 + bid * 4096 + cons * 16, 0, 16);
    STAMP(9);
    STAMP_FLUSH();
  }
}
}

extern "C" void kernel_launch(void* const* d_in, const int* in_sizes, int n_in, void* d_out, int out_size, void* d_ws, size_t ws_size,
                              hipStream_t stream) {
  (void)in_sizes; (void)n_in; (void)out_size; (void)ws_size;
  (void)hipFuncSetAttribute((const void*)mk, hipFuncAttributeMaxDynamicSharedMemorySize, L_ALLOC);
  mk<<<256, 512, L_ALLOC, stream>>>((const u16*)d_in[0], (const u16*)d_in[1], (const u16*)d_in[2], (const u16*)d_in[3], (const u16*)d_in[4],
                                  (const u16*)d_in[5], (const u16*)d_in[7], (const u16*)d_in[8], (const u16*)d_in[9], (const u16*)d_in[10],
                                  (u16*)d_out, (unsigned char*)d_ws);
}
```

```cpp
#include <hip/hip_runtime.h>
#include <hip/hip_bf16.h>
#include <stdint.h>

typedef unsigned short u16;
typedef unsigned v4u __attribute__((ext_vector_type(4)));
typedef unsigned v2u __attribute__((ext_vector_type(2)));
typedef float f32x4 __attribute__((ext_vector_type(4)));
typedef short s16x4 __attribute__((ext_vector_type(4)));
typedef __bf16 bf16x8 __attribute__((ext_vector_type(8)));
#define LAS __attribute__((address_space(3)))
#define GAS __attribute__((address_space(1)))

namespace {
constexpr int PITCH = 1056;
constexpr int L_KV = 0;
constexpr int L_Q = 128 * PITCH;
constexpr int L_P = L_Q + 16 * PITCH;
constexpr int L_SM = L_P + 4608;
constexpr int L_OL = L_Q;
constexpr int L_CTX = L_OL + 1024;
constexpr int L_PK = L_CTX + 4096;
constexpr int L_CNT = L_SM + 512;
constexpr int L_END = L_CNT + 64;
static_assert(L_END <= 160 * 1024, "LDS");
enum { C_S1 = 0, C_S2, C_KV, C_S3, C_S4, C_S5, C_CTX, C_ST, C_FIN, C_KVH, C_PUB, C_ISS };
constexpr unsigned OOB = 0xfffffff0u;

constexpr unsigned W_EPOCH = 0;
constexpr unsigned W_H2 = 4096;
constexpr unsigned W_HF = W_H2 + 2 * 2 * 32 * 512 * 4;
constexpr unsigned W_H4 = W_HF + 224 * 2048;
constexpr unsigned W_H3 = W_H4 + 256 * 256 * 16;
constexpr unsigned W_DBG = W_H3 + 32 * 8 * 2 * 64 * 64 * 4;
constexpr unsigned W_END = W_DBG + 256 * 2 * 16 * 8;
#define H3_SLEEP 8
#define H2_DELAY 20
#define H3_GATE 1
#define ISS_AT 20
#define TLBW 1
#define DMA0 8
#define WO_B 32
#define PRIO 3
#define HOLD2 0
#define KVH_N 20
#define WO_A 4
constexpr int SPIN_CAP = 1 << 15;
constexpr int LSPIN_CAP = 1 << 22;

__device__ __forceinline__ f32x4 mfma(v4u a, v4u b, f32x4 c) {
  return __builtin_amdgcn_mfma_f32_16x16x32_bf16(__builtin_bit_cast(bf16x8, a), __builtin_bit_cast(bf16x8, b), c, 0, 0, 0);
}
__device__ __forceinline__ float bf_lo(unsigned w) { return __uint_as_float(w << 16); }
__device__ __forceinline__ float bf_hi(unsigned w) { return __uint_as_float(w & 0xffff0000u); }
__device__ __forceinline__ unsigned pk_bf16(float a, float b) {
  typedef __bf16 bf16x2 __attribute__((ext_vector_type(2)));
  typedef float f32x2 __attribute__((ext_vector_type(2)));
  f32x2 v = {a, b};
  bf16x2 r = __builtin_convertvector(v, bf16x2);
  return __builtin_bit_cast(unsigned, r);
}
constexpr unsigned TMASK = 0xfffu;
__device__ __forceinline__ unsigned tagw(float v, unsigned tag) { return ((__float_as_uint(v) + 0x800u) & ~TMASK) | tag; }
__device__ __forceinline__ float untag(unsigned w) { return __uint_as_float(w & ~TMASK); }
__device__ __forceinline__ v4u tag4(f32x4 v, unsigned tag) { return v4u{tagw(v.x, tag), tagw(v.y, tag), tagw(v.z, tag), tagw(v.w, tag)}; }
__device__ __forceinline__ unsigned bad4(v4u w, unsigned tag) { return ((w.x ^ tag) | (w.y ^ tag) | (w.z ^ tag) | (w.w ^ tag)) & TMASK; }
template <int N> __device__ __forceinline__ float dpp_ror(float v) {
  return __int_as_float(__builtin_amdgcn_update_dpp(0, __float_as_int(v), 0x120 + N, 0xf, 0xf, false));
}
__device__ __forceinline__ float x16_add(float v) { auto p = __builtin_amdgcn_permlane16_swap(__float_as_uint(v), __float_as_uint(v), false, false); return __uint_as_float(p[0]) + __uint_as_float(p[1]); }
__device__ __forceinline__ float x32_add(float v) { auto p = __builtin_amdgcn_permlane32_swap(__float_as_uint(v), __float_as_uint(v), false, false); return __uint_as_float(p[0]) + __uint_as_float(p[1]); }
__device__ __forceinline__ float x16_max(float v) { auto p = __builtin_amdgcn_permlane16_swap(__float_as_uint(v), __float_as_uint(v), false, false); return fmaxf(__uint_as_float(p[0]), __uint_as_float(p[1])); }
__device__ __forceinline__ float x32_max(float v) { auto p = __builtin_amdgcn_permlane32_swap(__float_as_uint(v), __float_as_uint(v), false, false); return fmaxf(__uint_as_float(p[0]), __uint_as_float(p[1])); }
__device__ __forceinline__ float wave_sum(float v) { v += dpp_ror<8>(v); v += dpp_ror<4>(v); v += dpp_ror<2>(v); v += dpp_ror<1>(v); v = x16_add(v); return x32_add(v); }
__device__ __forceinline__ float wave_max(float v) { v = fmaxf(v, dpp_ror<8>(v)); v = fmaxf(v, dpp_ror<4>(v)); v = fmaxf(v, dpp_ror<2>(v)); v = fmaxf(v, dpp_ror<1>(v)); v = x16_max(v); return x32_max(v); }
__device__ __forceinline__ v4u ldnt(const u16* p) { return __builtin_nontemporal_load((const v4u*)p); }

#define LD128(off) (*(const LAS v4u*)(L + (off)))
#define LDF4(off) (*(const LAS f32x4*)(L + (off)))
#define FENCE() do { __builtin_amdgcn_sched_barrier(0); asm volatile("" ::: "memory"); } while (0)

#define STAMP(i)
#define STAMP_FLUSH()
#define L_ALLOC L_END

__global__ __launch_bounds__(512, 2) void mk(const u16* __restrict__ x, const u16* __restrict__ ckv, const u16* __restrict__ wq,
                                             const u16* __restrict__ wuk, const u16* __restrict__ wuv, const u16* __restrict__ wo,
                                             const u16* __restrict__ wg, const u16* __restrict__ wu, const u16* __restrict__ wd,
                                             const u16* __restrict__ gw, u16* __restrict__ out, unsigned char* ws) {
  extern __shared__ __attribute__((aligned(16))) unsigned char lds[];
  LAS unsigned char* L = (LAS unsigned char*)lds;
  const int tid = threadIdx.x, lane = tid & 63, wave = __builtin_amdgcn_readfirstlane(tid >> 6);
  const int bid = blockIdx.x, xcd = bid & 7, slot = bid >> 3;
  const int h = xcd * 4 + (slot >> 3), j = slot & 7;
  const int ab = xcd >> 2, as = (xcd & 3) * 16 + (slot >> 1), hg = slot & 1;
  const int g = lane >> 4, r = lane & 15, jj = r & 7;
  const auto rs = __builtin_amdgcn_make_buffer_rsrc(ws, 0, W_END, 0x00020000);
  STAMP(0);
  int vzero = 0;
  asm volatile("" : "+v"(vzero));
  unsigned selE[4], selO[4];
#pragma unroll
  for (int k = 0; k < 4; ++k) {
    selE[k] = (k == (jj >> 1)) ? ((jj & 1) ? 0x01000c0cu : 0x0c0c0100u) : 0x0c0c0c0cu;
    selO[k] = (k == (jj >> 1)) ? ((jj & 1) ? 0x03020c0cu : 0x0c0c0302u) : 0x0c0c0c0cu;
  }
  auto buildA = [&](unsigned pw, int odd) -> v4u {
    v4u a;
    if (odd) { a.x = __builtin_amdgcn_perm(pw, pw, selO[0]); a.y = __builtin_amdgcn_perm(pw, pw, selO[1]); a.z = __builtin_amdgcn_perm(pw, pw, selO[2]); a.w = __builtin_amdgcn_perm(pw, pw, selO[3]); }
    else     { a.x = __builtin_amdgcn_perm(pw, pw, selE[0]); a.y = __builtin_amdgcn_perm(pw, pw, selE[1]); a.z = __builtin_amdgcn_perm(pw, pw, selE[2]); a.w = __builtin_amdgcn_perm(pw, pw, selE[3]); }
    return a;
  };
  auto arrive = [&](int c) {
    FENCE();
    if (lane == 0) __hip_atomic_fetch_add((LAS unsigned*)(L + L_CNT + 4 * c), 1u, __ATOMIC_RELAXED, __HIP_MEMORY_SCOPE_WORKGROUP);
    FENCE();
  };
  auto await = [&](int c, unsigned n) {
    FENCE();
    for (int s = 0; s < LSPIN_CAP; ++s) {
      if (*(volatile LAS unsigned*)(L + L_CNT + 4 * c) >= n) break;
    }
    FENCE();
  };

  auto arrive_a = [&](int c) {
    FENCE();
    if (lane == 0) asm volatile("ds_add_u32 %0, %1" ::"v"((unsigned)(L_CNT + 4 * c)), "v"(1u) : "memory");
    FENCE();
  };
  auto await_a = [&](int c, unsigned n) {
    FENCE();
    for (int s = 0; s < LSPIN_CAP; ++s) {
      unsigned v;
      asm volatile("ds_read_b32 %0, %1\n\ts_waitcnt lgkmcnt(0)" : "=v"(v) : "v"((unsigned)(L_CNT + 4 * c)) : "memory");
      if ((unsigned)__builtin_amdgcn_readfirstlane((int)v) >= n) break;
    }
    FENCE();
  };
#define GATHER(N, v, base, stride, tagv) GATHERX(N, v, base, stride, tagv, , 0u)
#define GATHERX(N, v, base, stride, tagv, EXTRA_LOAD, EXTRA_BAD)                                                \
  {                                                                                                             \
    unsigned pend_ = (1u << (N)) - 1u;                                                                          \
    for (int spin_ = 0; spin_ < SPIN_CAP; ++spin_) {                                                            \
      asm volatile("" ::: "memory");                                                                            \
      v4u t_[N];                                                                                                \
      EXTRA_LOAD;                                                                                               \
      _Pragma("unroll") for (int i_ = 0; i_ < (N); ++i_)                                                        \
        t_[i_] = __builtin_amdgcn_raw_buffer_load_b128(rs, ((pend_ >> i_) & 1u) ? (base) + i_ * (stride) : OOB, 0, 16); \
      _Pragma("unroll") for (int i_ = 0; i_ < (N); ++i_) {                                                      \
        const bool p_ = (pend_ >> i_) & 1u;                                                                     \
        v[i_].x = p_ ? t_[i_].x : v[i_].x; v[i_].y = p_ ? t_[i_].y : v[i_].y;                                   \
        v[i_].z = p_ ? t_[i_].z : v[i_].z; v[i_].w = p_ ? t_[i_].w : v[i_].w;                                   \
        if (bad4(v[i_], tagv) == 0) pend_ &= ~(1u << i_);                                                       \
      }                                                                                                         \
      if (__all((pend_ | (EXTRA_BAD)) == 0)) break;                                                             \
      __builtin_amdgcn_s_sleep(1);                                                                              \
    }                                                                                                           \
  }

  if (wave < 4) {
    const int w = wave;
    __builtin_amdgcn_s_setprio(PRIO);
    unsigned tag, epoch;
    {
      const int dq = j & 1, cq = j >> 1;
      const u16* wkb = wuk + ((size_t)(128 * cq + 32 * w + r) * 32 + h) * 128 + 64 * dq + 8 * g;
      v4u wk[4];
#pragma unroll
      for (int f = 0; f < 4; ++f) wk[f] = ldnt(wkb + (size_t)(f >> 1) * (16 * 4096) + 32 * (f & 1));
      const u16* xp = x + (r >> 3) * 512 + 128 * w + 32 * g;
      v2u xv[8];
#pragma unroll
      for (int k = 0; k < 8; ++k) xv[k] = *(const v2u*)(xp + 16 * (k >> 2) + ((4 * k + 4 * cq) & 15));
      const u16* wqb = wq + (size_t)(128 * w + 32 * g + 16 * (r >> 3)) * 4096 + h * 128 + 64 * dq + 8 * (r & 7);
      v4u wv[16];
#pragma unroll
      for (int i = 0; i < 16; ++i) wv[i] = ldnt(wqb + (size_t)((i + 4 * cq) & 15) * 4096);
      epoch = __builtin_amdgcn_raw_buffer_load_b32(rs, vzero + W_EPOCH, 0, 16);
      FENCE();
      __builtin_amdgcn_s_barrier();
      FENCE();
      f32x4 acc = {0.f, 0.f, 0.f, 0.f}, acc2 = {0.f, 0.f, 0.f, 0.f};
#pragma unroll
      for (int i = 0; i < 16; ++i) {
        acc = mfma(buildA(xv[i >> 2][(i >> 1) & 1], i & 1), wv[i], acc);
        acc2 = mfma(buildA(xv[4 + (i >> 2)][(i >> 1) & 1], i & 1), wv[i], acc2);
      }
#pragma unroll
      for (int e = 0; e < 4; ++e) { float v = (r < 8) ? acc[e] : acc2[e]; acc[e] = v + dpp_ror<8>(v); }
      STAMP(1);
      if (r < 8) {
        const unsigned h01 = pk_bf16(acc.x, acc.y), h23 = pk_bf16(acc.z, acc.w);
        const unsigned l01 = pk_bf16(acc.x - bf_lo(h01), acc.y - bf_hi(h01)), l23 = pk_bf16(acc.z - bf_lo(h23), acc.w - bf_hi(h23));
        const int b = g >> 1, d0 = 8 * r + 4 * (g & 1);
        *(LAS v2u*)(L + L_P + (b + 4 * w) * 160 + d0 * 2) = v2u{h01, h23};
        *(LAS v2u*)(L + L_P + (b + 2 + 4 * w) * 160 + d0 * 2) = v2u{l01, l23};
      }
      arrive(C_S1);
      await(C_S1, 4);
      const v4u qb0 = LD128(L_P + r * 160 + g * 16), qb1 = LD128(L_P + r * 160 + 64 + g * 16);
      f32x4 qa = {0.f, 0.f, 0.f, 0.f}, qc = {0.f, 0.f, 0.f, 0.f};
      qa = mfma(wk[0], qb0, qa); qc = mfma(wk[2], qb0, qc);
      qa = mfma(wk[1], qb1, qa); qc = mfma(wk[3], qb1, qc);
#pragma unroll
      for (int e = 0; e < 4; ++e) {
        float v = qa[e]; v += dpp_ror<8>(v); v += dpp_ror<4>(v); v += dpp_ror<2>(v);
        float u = qc[e]; u += dpp_ror<8>(u); u += dpp_ror<4>(u); u += dpp_ror<2>(u);
        qa[e] = ((r & 2) ? u : v) * (0.08838834764831845f * 1.4426950408889634f);
      }
      tag = (epoch + 1u) & TMASK;
      if (r < 4)
        __builtin_amdgcn_raw_buffer_store_b128(tag4(qa, tag), rs,
            W_H2 + (((((r & 1) * 2 + (h >> 4)) * 2 + dq) * 16 + (h & 15)) * 512 + 128 * cq + 32 * w + 16 * (r >> 1) + 4 * g) * 4, 0, 16);
    }
    STAMP(2);
    await(C_ISS, 4);
    STAMP(3);
    v4u kf[32];
    {
      v4u v[16];
      const unsigned base = W_H2 + (unsigned)(ab * 2 + hg) * 65536u + (unsigned)tid * 16u;
#pragma unroll
      for (int i = 0; i < 16; ++i) v[i] = __builtin_amdgcn_raw_buffer_load_b128(rs, base + i * 4096u, 0, 16);
      await(C_KV, 4);
      STAMP(12);
#pragma unroll
      for (int ks = 0; ks < 16; ++ks) {
        kf[2 * ks] = LD128(L_KV + (32 * w + r) * PITCH + ks * 64 + g * 16);
        kf[2 * ks + 1] = LD128(L_KV + (32 * w + 16 + r) * PITCH + ks * 64 + g * 16);
      }
      FENCE();
      unsigned bad0 = 0;
#pragma unroll
      for (int i = 0; i < 16; ++i) bad0 |= bad4(v[i], tag);
      if (!__all(bad0 == 0)) {
        for (int spin = 0; spin < SPIN_CAP; ++spin) {
          asm volatile("" ::: "memory");
#pragma unroll
          for (int i = 0; i < 16; ++i) v[i] = __builtin_amdgcn_raw_buffer_load_b128(rs, base + i * 4096u, 0, 16);
          unsigned bad = 0;
#pragma unroll
          for (int i = 0; i < 16; ++i) bad |= bad4(v[i], tag);
          if (__all(bad == 0)) break;
        }
        FENCE();
#pragma unroll
        for (int ks = 0; ks < 16; ++ks) {
          kf[2 * ks] = LD128(L_KV + (32 * w + r) * PITCH + ks * 64 + g * 16);
          kf[2 * ks + 1] = LD128(L_KV + (32 * w + 16 + r) * PITCH + ks * 64 + g * 16);
        }
      }
#pragma unroll
      for (int i = 0; i < 8; ++i) {
        const int head = 2 * i + (tid >> 7), c = 4 * (tid & 127);
        *(LAS v2u*)(L + L_Q + head * PITCH + c * 2) =
            v2u{pk_bf16(untag(v[i].x) + untag(v[i + 8].x), untag(v[i].y) + untag(v[i + 8].y)), pk_bf16(untag(v[i].z) + untag(v[i + 8].z), untag(v[i].w) + untag(v[i + 8].w))};
      }
    }
    STAMP(11);
    arrive(C_S2);
    v4u wvv[4];
    {
      const u16* wvb = wuv + ((size_t)(64 * j + 16 * w + 4 * g) * 32 + h) * 128 + 8 * r;
#pragma unroll
      for (int i = 0; i < 4; ++i) wvv[i] = ldnt(wvb + (size_t)i * 4096);
    }
    await(C_S2, 4);
    STAMP(4);
    {
      f32x4 s0 = {0.f, 0.f, 0.f, 0.f}, s1 = {0.f, 0.f, 0.f, 0.f};
      const int tq = r >> 2, tp = r & 3;
      v4u vf[32];
      {
        v4u qf[16];
#pragma unroll
        for (int ks = 0; ks < 16; ++ks) qf[ks] = LD128(L_Q + r * PITCH + ks * 64 + g * 16);
        FENCE();
#pragma unroll
        for (int ks = 0; ks < 16; ++ks) {
          s0 = mfma(kf[2 * ks], qf[ks], s0);
          s1 = mfma(kf[2 * ks + 1], qf[ks], s1);
#pragma unroll
          for (int e = 0; e < 2; ++e) {
            const int ct = ks >> 1, i = 2 * (ks & 1) + e;
            const int a0 = L_KV + (32 * i + 4 * g + tq) * PITCH + (128 * w + 16 * ct + 4 * tp) * 2;
            const v2u t0 = __builtin_bit_cast(v2u, __builtin_amdgcn_ds_read_tr16_b64_v4i16((LAS s16x4*)(L + a0)));
            const v2u t1 = __builtin_bit_cast(v2u, __builtin_amdgcn_ds_read_tr16_b64_v4i16((LAS s16x4*)(L + a0 + 16 * PITCH)));
            vf[4 * ct + i] = v4u{t0.x, t0.y, t1.x, t1.y};
          }
          __builtin_amdgcn_sched_barrier(0);
        }
      }
      FENCE();
      float mx = fmaxf(fmaxf(fmaxf(s0.x, s0.y), fmaxf(s0.z, s0.w)), fmaxf(fmaxf(s1.x, s1.y), fmaxf(s1.z, s1.w)));
      mx = x32_max(x16_max(mx));
      if (g == 0) *(LAS float*)(L + L_SM + (r * 4 + w) * 4) = mx;
      arrive(C_S3);
      await(C_S3, 4);
      const f32x4 m0 = LDF4(L_SM + r * 16);
      float M = fmaxf(fmaxf(m0.x, m0.y), fmaxf(m0.z, m0.w));
      f32x4 p0, p1;
#pragma unroll
      for (int e = 0; e < 4; ++e) { p0[e] = __builtin_amdgcn_exp2f(s0[e] - M); p1[e] = __builtin_amdgcn_exp2f(s1[e] - M); }
      *(LAS v4u*)(L + L_P + w * 1024 + lane * 16) = v4u{pk_bf16(p0.x, p0.y), pk_bf16(p0.z, p0.w), pk_bf16(p1.x, p1.y), pk_bf16(p1.z, p1.w)};
      float ls = ((p0.x + p0.y) + (p0.z + p0.w)) + ((p1.x + p1.y) + (p1.z + p1.w));
      ls = x32_add(x16_add(ls));
      if (g == 0) *(LAS float*)(L + L_SM + 256 + (r * 4 + w) * 4) = ls;
      arrive(C_S4);
      await(C_S4, 4);
      STAMP(5);
      v4u pf[4];
#pragma unroll
      for (int i = 0; i < 4; ++i) pf[i] = LD128(L_P + i * 1024 + lane * 16);
      unsigned lsb;
      float osc;
      {
        const f32x4 l0 = LDF4(L_SM + 256 + r * 16);
        const float lse = M + __builtin_amdgcn_logf((l0.x + l0.y) + (l0.z + l0.w));
        lsb = (__float_as_uint(lse) + 0x80u) & 0xffffff00u;
        osc = __builtin_amdgcn_exp2f(M - __uint_as_float(lsb));
      }
      const unsigned f1 = lsb >> 20, f3 = (lsb >> 8) & TMASK;
      const int stg = L_KV + w * (32 * 272);
#pragma unroll
      for (int ct = 0; ct < 8; ++ct) {
        f32x4 o = {0.f, 0.f, 0.f, 0.f};
#pragma unroll
        for (int i = 0; i < 4; ++i) o = mfma(vf[4 * ct + i], pf[i], o);
        o *= osc;
        *(LAS v4u*)(L + stg + ((ct >> 2) * 16 + r) * 272 + ((ct & 3) * 4 + g) * 16) = v4u{tagw(o.x, tag), tagw(o.y, f1), tagw(o.z, tag), tagw(o.w, f3)};
      }
      FENCE();
#pragma unroll
      for (int k = 0; k < 8; ++k) {
        const v4u d = LD128(stg + (4 * k + g) * 272 + r * 16);
        __builtin_amdgcn_raw_buffer_store_b128(d, rs, W_H3 + (((ab * 64 + as) * 2 + hg) * 32 + 8 * w + k) * 1024 + lane * 16, 0, 16);
      }
    }
    arrive(C_ST);
    STAMP(6);
    await(C_KVH, 4);
    {
      const int b = w >> 1;
      const int c4 = tid & 15, sg = (tid >> 4) & 7;
      v4u v[8] = {};
      const unsigned obase = W_H3 + (unsigned)(((b * 64 + sg) * 2 + (h >> 4)) * 32 + 4 * j) * 1024u + (h & 15) * 256u + c4 * 16u;
      {
        unsigned pend = 0xffu;
        for (int spin = 0; spin < SPIN_CAP; ++spin) {
          asm volatile("" ::: "memory");
          v4u t[8];
#pragma unroll
          for (int i = 0; i < 8; ++i) t[i] = __builtin_amdgcn_raw_buffer_load_b128(rs, ((pend >> i) & 1u) ? obase + i * (8u * 65536u) : OOB, 0, 16);
          unsigned badm = 0;
#pragma unroll
          for (int i = 0; i < 8; ++i) badm |= ((((t[i].x ^ tag) | (t[i].z ^ tag)) & TMASK) ? 1u : 0u) << i;
          badm |= (unsigned)__builtin_amdgcn_update_dpp(0, (int)badm, 0x128, 0xf, 0xf, false);
          badm |= (unsigned)__builtin_amdgcn_update_dpp(0, (int)badm, 0x124, 0xf, 0xf, false);
          badm |= (unsigned)__builtin_amdgcn_update_dpp(0, (int)badm, 0x122, 0xf, 0xf, false);
          badm |= (unsigned)__builtin_amdgcn_update_dpp(0, (int)badm, 0x121, 0xf, 0xf, false);
#pragma unroll
          for (int i = 0; i < 8; ++i) {
            const bool p = (pend >> i) & 1u;
            v[i].x = p ? t[i].x : v[i].x; v[i].y = p ? t[i].y : v[i].y; v[i].z = p ? t[i].z : v[i].z; v[i].w = p ? t[i].w : v[i].w;
          }
          pend &= badm;
          if (__all(pend == 0)) break;
          __builtin_amdgcn_s_sleep(H3_SLEEP);
        }
      }
      STAMP(7);
      float ls[8];
      float Mw = -3.0e38f;
#pragma unroll
      for (int i = 0; i < 8; ++i) { ls[i] = __uint_as_float(((v[i].y & TMASK) << 20) | ((v[i].w & TMASK) << 8)); Mw = fmaxf(Mw, ls[i]); }
      Mw = x32_max(x16_max(Mw));
      f32x4 o4 = {0.f, 0.f, 0.f, 0.f};
      float Lw = 0.f;
#pragma unroll
      for (int i = 0; i < 8; ++i) {
        const float ws_ = __builtin_amdgcn_exp2f(ls[i] - Mw);
        Lw += ws_;
        o4.x += ws_ * untag(v[i].x); o4.y += ws_ * untag(v[i].y); o4.z += ws_ * untag(v[i].z); o4.w += ws_ * untag(v[i].w);
      }
#pragma unroll
      for (int e2 = 0; e2 < 4; ++e2) o4[e2] = x32_add(x16_add(o4[e2]));
      Lw = x32_add(x16_add(Lw));
      if (g == 0) *(LAS f32x4*)(L + L_OL + w * 256 + c4 * 16) = o4;
      if (lane == 0) *(LAS v2u*)(L + L_SM + w * 8) = v2u{__float_as_uint(Mw), __float_as_uint(Lw)};
    }
    arrive(C_S5);
    await(C_S5, 4);
    {
      const int b = r >> 3;
      const f32x4 oa = LDF4(L_OL + (2 * b) * 256 + (16 * w + 4 * g) * 4), ob = LDF4(L_OL + (2 * b + 1) * 256 + (16 * w + 4 * g) * 4);
      const f32x4 ml = LDF4(L_SM + b * 16);
      const float Mb = fmaxf(ml.x, ml.z);
      const float ea = __builtin_amdgcn_exp2f(ml.x - Mb), eb = __builtin_amdgcn_exp2f(ml.z - Mb);
      const float inv = 1.f / (ml.y * ea + ml.w * eb);
      const f32x4 ov = oa * (ea * inv) + ob * (eb * inv);
      const unsigned h01 = pk_bf16(ov.x, ov.y), h23 = pk_bf16(ov.z, ov.w);
      const unsigned l01 = pk_bf16(ov.x - bf_lo(h01), ov.y - bf_hi(h01)), l23 = pk_bf16(ov.z - bf_lo(h23), ov.w - bf_hi(h23));
      f32x4 c0 = {0.f, 0.f, 0.f, 0.f}, c1 = {0.f, 0.f, 0.f, 0.f};
      c0 = mfma(buildA(h01, 0), wvv[0], c0); c1 = mfma(buildA(l01, 0), wvv[0], c1);
      c0 = mfma(buildA(h01, 1), wvv[1], c0); c1 = mfma(buildA(l01, 1), wvv[1], c1);
      c0 = mfma(buildA(h23, 0), wvv[2], c0); c1 = mfma(buildA(l23, 0), wvv[2], c1);
      c0 = mfma(buildA(h23, 1), wvv[3], c0); c1 = mfma(buildA(l23, 1), wvv[3], c1);
      *(LAS f32x4*)(L + L_CTX + w * 1024 + (g >> 1) * 512 + (8 * r + 4 * (g & 1)) * 4) = c0 + c1;
    }
    arrive(C_CTX);
    STAMP(8);
    if ((bid & 3) == 0) {
      const int oc = bid >> 2;
      v4u q[4] = {};
      const unsigned qbase = W_H4 + (unsigned)(64 * w + (lane >> 2)) * 4096u + oc * 64u + (lane & 3) * 16u;
      GATHER(4, q, qbase, 16u * 4096u, tag);
      STAMP(9);
      f32x4 t4;
#pragma unroll
      for (int e2 = 0; e2 < 4; ++e2) {
        float t = (untag(q[0][e2]) + untag(q[1][e2])) + (untag(q[2][e2]) + untag(q[3][e2]));
        t += dpp_ror<8>(t); t += dpp_ror<4>(t);
        t4[e2] = x32_add(x16_add(t));
      }
      if (lane < 4) *(LAS f32x4*)(L + L_SM + 256 + w * 64 + lane * 16) = t4;
      arrive(C_FIN);
      if (w == 0) {
        await(C_FIN, 4);
        if (lane < 4) {
          const f32x4 o = (LDF4(L_SM + 256 + lane * 16) + LDF4(L_SM + 320 + lane * 16)) + (LDF4(L_SM + 384 + lane * 16) + LDF4(L_SM + 448 + lane * 16));
          *(v2u*)(out + oc * 16 + lane * 4) = v2u{pk_bf16(o.x, o.y), pk_bf16(o.z, o.w)};
          if (bid == 0 && lane == 0) __builtin_amdgcn_raw_buffer_store_b32(epoch + 1u, rs, W_EPOCH, 0, 16);
        }
      }
    }
    STAMP(10);
    STAMP_FLUSH();
  } else {
    const int w = wave - 4;
    if (w == 0 && lane < 16) *(LAS unsigned*)(L + L_CNT + 4 * lane) = 0u;
    asm volatile("s_waitcnt lgkmcnt(0)" ::: "memory");
    FENCE();
    __builtin_amdgcn_s_barrier();
    FENCE();
    const u16* src = ckv + ((size_t)ab * 8192 + as * 128 + w * 32) * 512 + lane * 8;
#pragma unroll
    for (int i = 0; i < 32; ++i)
      __builtin_amdgcn_global_load_lds((const void*)(src + i * 512), (LAS void*)(L + L_KV + (w * 32 + i) * PITCH), 16, 0, 2);
    arrive_a(C_ISS);
    __builtin_amdgcn_s_waitcnt(0x0f70);
    arrive_a(C_KV);
    STAMP(1);
    await_a(C_S2, 4);
    v4u fv[8]; v4u fx;
    const bool ffn1 = (w < 2) && (bid < 224);
    const int cb = bid % 14, rb = bid / 14;
    if (ffn1) {
      const u16* fb = (w == 0 ? wg : wu) + (size_t)(32 * rb + 8 * g) * 1792 + 128 * cb + 8 * r;
#pragma unroll
      for (int i = 0; i < 8; ++i) fv[i] = ldnt(fb + (size_t)i * 1792);
      fx = *(const v4u*)(x + (r >> 3) * 512 + 32 * rb + 8 * g);
    }
    const v4u gx0 = *(const v4u*)(x + lane * 8), gx1 = *(const v4u*)(x + 512 + lane * 8), gwv = *(const v4u*)(gw + lane * 8);
    const unsigned epoch = __builtin_amdgcn_raw_buffer_load_b32(rs, vzero + W_EPOCH, 0, 16);
    v4u wov[34];
    const u16* wob = wo + ((size_t)h * 128 + 32 * g) * 512 + 128 * w + 8 * r;
#pragma unroll
    for (int i = 0; i < WO_A; ++i) wov[i] = ldnt(wob + ((i + 4 * j) & 31) * 512);
    {
      const int u = bid < 224 ? bid : 0;
      const u16* wdb = wd + (size_t)(8 * u + 2 * g) * 512 + 128 * w + 8 * r;
      wov[32] = ldnt(wdb);
      wov[33] = ldnt(wdb + 512);
    }
    FENCE();
    const unsigned tag = (epoch + 1u) & TMASK;
    if (ffn1) {
      f32x4 acc = {0.f, 0.f, 0.f, 0.f};
#pragma unroll
      for (int i = 0; i < 8; ++i) acc = mfma(buildA(fx[i >> 1], i & 1), fv[i], acc);
      const unsigned u = 16 * cb + r;
      __builtin_amdgcn_raw_buffer_store_b128(tag4(acc, tag), rs, W_HF + u * 2048 + ((w * 16 + rb) * 4 + g) * 16, 0, 16);
    }
    float gate0, gate1;
    {
      float s0 = 0.f, s1 = 0.f;
#pragma unroll
      for (int k = 0; k < 4; ++k) {
        s0 += bf_lo(gx0[k]) * bf_lo(gwv[k]) + bf_hi(gx0[k]) * bf_hi(gwv[k]);
        s1 += bf_lo(gx1[k]) * bf_lo(gwv[k]) + bf_hi(gx1[k]) * bf_hi(gwv[k]);
      }
      s0 = wave_sum(s0); s1 = wave_sum(s1);
      gate0 = 1.f / (1.f + __expf(-s0));
      gate1 = 1.f / (1.f + __expf(-s1));
    }
    STAMP(2);
    await_a(C_ST, 4);
    STAMP(5);
#pragma unroll
    for (int i = WO_A; i < 32; ++i) wov[i] = ldnt(wob + ((i + 4 * j) & 31) * 512);
    arrive_a(C_KVH);
    STAMP(6);
    unsigned hp = 0u, lp = 0u;
    if (bid < 224) {
      v4u ff[2] = {};
      const unsigned fbase = W_HF + bid * 2048 + lane * 16;
      GATHER(2, ff, fbase, 1024u, tag);
      const v4u f0 = ff[0], f1 = ff[1];
      f32x4 hv;
      const float og = 1.f - (((lane >> 1) & 1) ? gate1 : gate0);
#pragma unroll
      for (int e2 = 0; e2 < 4; ++e2) {
        float t = untag(f0[e2]); t += dpp_ror<8>(t); t += dpp_ror<4>(t); t = x32_add(x16_add(t));
        float u = untag(f1[e2]); u += dpp_ror<8>(u); u += dpp_ror<4>(u); u = x32_add(x16_add(u));
        const float a2 = 2.f * 0.7978845608028654f * (t + 0.044715f * t * t * t);
        hv[e2] = t / (1.f + __expf(-a2)) * u * og;
      }
      const int srcl = (r >> 3) * 2 + (g >> 1);
      const float a0 = __shfl(hv.x, srcl), a1 = __shfl(hv.y, srcl), a2_ = __shfl(hv.z, srcl), a3 = __shfl(hv.w, srcl);
      const float h0 = (g & 1) ? a2_ : a0, h1 = (g & 1) ? a3 : a1;
      hp = pk_bf16(h0, h1);
      lp = pk_bf16(h0 - bf_lo(hp), h1 - bf_hi(hp));
    }
    f32x4 acc = {0.f, 0.f, 0.f, 0.f};
    acc = mfma(buildA(hp, 0), wov[32], acc);
    acc = mfma(buildA(hp, 1), wov[33], acc);
    acc = mfma(buildA(lp, 0), wov[32], acc);
    acc = mfma(buildA(lp, 1), wov[33], acc);
    STAMP(7);
    await_a(C_CTX, 4);
    STAMP(8);
    {
      const int b = r >> 3;
      const float gb = b ? gate1 : gate0;
      const int off = L_CTX + b * 512 + (32 * g + 4 * jj) * 4;
      const f32x4 cs = ((LDF4(off) + LDF4(off + 1024)) + (LDF4(off + 2048) + LDF4(off + 3072))) * gb;
      *(LAS v2u*)(L + L_PK + w * 512 + b * 256 + (16 * g + 2 * jj) * 4) = v2u{pk_bf16(cs.x, cs.y), pk_bf16(cs.z, cs.w)};
      FENCE();
      v2u pk[8];
#pragma unroll
      for (int k = 0; k < 8; ++k) pk[k] = *(const LAS v2u*)(L + L_PK + w * 512 + b * 256 + (16 * g + ((2 * k + 2 * j) & 15)) * 4);
#pragma unroll
      for (int i = 0; i < 32; ++i) acc = mfma(buildA(pk[i >> 2][(i >> 1) & 1], i & 1), wov[i], acc);
    }
    const unsigned cons = (g >> 1) * 128 + 32 * w + 2 * r + (g & 1);
    __builtin_amdgcn_raw_buffer_store_b128(tag4(acc, tag), rs, W_H4 + bid * 4096 + cons * 16, 0, 16);
    STAMP(9);
    STAMP_FLUSH();
  }
}
}

extern "C" void kernel_launch(void* const* d_in, const int* in_sizes, int n_in, void* d_out, int out_size, void* d_ws, size_t ws_size,
                              hipStream_t stream) {
  (void)in_sizes; (void)n_in; (void)out_size; (void)ws_size;
  (void)hipFuncSetAttribute((const void*)mk, hipFuncAttributeMaxDynamicSharedMemorySize, L_ALLOC);
  mk<<<256, 512, L_ALLOC, stream>>>((const u16*)d_in[0], (const u16*)d_in[1], (const u16*)d_in[2], (const u16*)d_in[3], (const u16*)d_in[4],
                                  (const u16*)d_in[5], (const u16*)d_in[7], (const u16*)d_in[8], (const u16*)d_in[9], (const u16*)d_in[10],
                                  (u16*)d_out, (unsigned char*)d_ws);
}
```

```cpp
#define TG 2.5f
#define DIAG 2
#include <hip/hip_runtime.h>
#include <hip/hip_bf16.h>
#include <stdint.h>

typedef unsigned short u16;
typedef unsigned v4u __attribute__((ext_vector_type(4)));
typedef unsigned v2u __attribute__((ext_vector_type(2)));
typedef float f32x4 __attribute__((ext_vector_type(4)));
typedef short s16x4 __attribute__((ext_vector_type(4)));
typedef __bf16 bf16x8 __attribute__((ext_vector_type(8)));
#define LAS __attribute__((address_space(3)))
#define GAS __attribute__((address_space(1)))

namespace {
constexpr int PITCH = 1056;
constexpr int L_KV = 0;
constexpr int L_Q = 128 * PITCH;
constexpr int L_P = L_Q + 16 * PITCH;
constexpr int L_SM = L_P + 4608;
constexpr int L_OL = L_Q;
constexpr int L_CTX = L_OL + 1024;
constexpr int L_PK = L_CTX + 4096;
constexpr int L_WV = L_KV + 65536;
constexpr int L_CNT = L_SM + 512;
constexpr int L_END = L_CNT + 64;
static_assert(L_END <= 160 * 1024, "LDS");
enum { C_S1 = 0, C_S2, C_KV, C_S3, C_S4, C_S5, C_CTX, C_ST, C_FIN, C_KVH, C_PUB, C_ISS };
constexpr unsigned OOB = 0xfffffff0u;

constexpr unsigned W_EPOCH = 0;
constexpr unsigned W_H2 = 4096;
constexpr unsigned W_HF = W_H2 + 2 * 2 * 32 * 512 * 4;
constexpr unsigned W_H4 = W_HF + 224 * 2048;
constexpr unsigned W_H3 = W_H4 + 256 * 256 * 16;
constexpr unsigned W_DBG = W_H3 + 32 * 8 * 2 * 64 * 64 * 4;
constexpr unsigned W_REG = W_DBG + 256 * 2 * 16 * 8;
#define NROT 1
constexpr unsigned W_END = W_REG * NROT;
#define H3_SLEEP 8
#define H2_DELAY 20
#define H3_GATE 0
#define ISS_AT 20
#define TLBW 1
#define DMA0 8
#define WO_B 32
#define PRIO 3
#define HOLD2 0
#define H3_DELAY 30
#define H4_DELAY 45
#define UNIQ_S2 1
#define GTRIG 1
#define KVH_N 20
#define WO_A 4
constexpr int SPIN_CAP = 1 << 15;
constexpr int LSPIN_CAP = 1 << 22;

__device__ __forceinline__ f32x4 mfma(v4u a, v4u b, f32x4 c) {
  return __builtin_amdgcn_mfma_f32_16x16x32_bf16(__builtin_bit_cast(bf16x8, a), __builtin_bit_cast(bf16x8, b), c, 0, 0, 0);
}
__device__ __forceinline__ float bf_lo(unsigned w) { return __uint_as_float(w << 16); }
__device__ __forceinline__ float bf_hi(unsigned w) { return __uint_as_float(w & 0xffff0000u); }
__device__ __forceinline__ unsigned pk_bf16(float a, float b) {
  typedef __bf16 bf16x2 __attribute__((ext_vector_type(2)));
  typedef float f32x2 __attribute__((ext_vector_type(2)));
  f32x2 v = {a, b};
  bf16x2 r = __builtin_convertvector(v, bf16x2);
  return __builtin_bit_cast(unsigned, r);
}
constexpr unsigned TMASK = 0xfffu;
__device__ __forceinline__ unsigned tagw(float v, unsigned tag) { return ((__float_as_uint(v) + 0x800u) & ~TMASK) | tag; }
__device__ __forceinline__ float untag(unsigned w) { return __uint_as_float(w & ~TMASK); }
__device__ __forceinline__ v4u tag4(f32x4 v, unsigned tag) { return v4u{tagw(v.x, tag), tagw(v.y, tag), tagw(v.z, tag), tagw(v.w, tag)}; }
__device__ __forceinline__ unsigned bad4(v4u w, unsigned tag) { return ((w.x ^ tag) | (w.y ^ tag) | (w.z ^ tag) | (w.w ^ tag)) & TMASK; }
template <int N> __device__ __forceinline__ float dpp_ror(float v) {
  return __int_as_float(__builtin_amdgcn_update_dpp(0, __float_as_int(v), 0x120 + N, 0xf, 0xf, false));
}
__device__ __forceinline__ float x16_add(float v) { auto p = __builtin_amdgcn_permlane16_swap(__float_as_uint(v), __float_as_uint(v), false, false); return __uint_as_float(p[0]) + __uint_as_float(p[1]); }
__device__ __forceinline__ float x32_add(float v) { auto p = __builtin_amdgcn_permlane32_swap(__float_as_uint(v), __float_as_uint(v), false, false); return __uint_as_float(p[0]) + __uint_as_float(p[1]); }
__device__ __forceinline__ float x16_max(float v) { auto p = __builtin_amdgcn_permlane16_swap(__float_as_uint(v), __float_as_uint(v), false, false); return fmaxf(__uint_as_float(p[0]), __uint_as_float(p[1])); }
__device__ __forceinline__ float x32_max(float v) { auto p = __builtin_amdgcn_permlane32_swap(__float_as_uint(v), __float_as_uint(v), false, false); return fmaxf(__uint_as_float(p[0]), __uint_as_float(p[1])); }
__device__ __forceinline__ float wave_sum(float v) { v += dpp_ror<8>(v); v += dpp_ror<4>(v); v += dpp_ror<2>(v); v += dpp_ror<1>(v); v = x16_add(v); return x32_add(v); }
__device__ __forceinline__ float wave_max(float v) { v = fmaxf(v, dpp_ror<8>(v)); v = fmaxf(v, dpp_ror<4>(v)); v = fmaxf(v, dpp_ror<2>(v)); v = fmaxf(v, dpp_ror<1>(v)); v = x16_max(v); return x32_max(v); }
__device__ __forceinline__ v4u ldnt(const u16* p) { return __builtin_nontemporal_load((const v4u*)p); }

#define LD128(off) (*(const LAS v4u*)(L + (off)))
#define LDF4(off) (*(const LAS f32x4*)(L + (off)))
#define FENCE() do { __builtin_amdgcn_sched_barrier(0); asm volatile("" ::: "memory"); } while (0)

#define STAMP(i)
#define STAMP_FLUSH()
#define L_ALLOC L_END

__global__ __launch_bounds__(512, 2) void mk(const u16* __restrict__ x, const u16* __restrict__ ckv, const u16* __restrict__ wq,
                                             const u16* __restrict__ wuk, const u16* __restrict__ wuv, const u16* __restrict__ wo,
                                             const u16* __restrict__ wg, const u16* __restrict__ wu, const u16* __restrict__ wd,
                                             const u16* __restrict__ gw, u16* __restrict__ out, unsigned char* ws) {
  extern __shared__ __attribute__((aligned(16))) unsigned char lds[];
  LAS unsigned char* L = (LAS unsigned char*)lds;
  const int tid = threadIdx.x, lane = tid & 63, wave = __builtin_amdgcn_readfirstlane(tid >> 6);
  const int bid = blockIdx.x, xcd = bid & 7, slot = bid >> 3;
  const int h = xcd * 4 + (slot >> 3), j = slot & 7;
  const int ab = xcd >> 2, as = (xcd & 3) * 16 + (slot >> 1), hg = slot & 1;
  const int g = lane >> 4, r = lane & 15, jj = r & 7;
  const auto rs = __builtin_amdgcn_make_buffer_rsrc(ws, 0, W_END, 0x00020000);
  STAMP(0);
  const unsigned long long tstart_ = __builtin_amdgcn_s_memrealtime();
  if (tid == 0) __builtin_amdgcn_raw_buffer_store_b64(v2u{(unsigned)tstart_, (unsigned)(tstart_ >> 32)}, rs, W_DBG + bid * 8, 0, 16);
  int vzero = 0;
  asm volatile("" : "+v"(vzero));
  unsigned selE[4], selO[4];
#pragma unroll
  for (int k = 0; k < 4; ++k) {
    selE[k] = (k == (jj >> 1)) ? ((jj & 1) ? 0x01000c0cu : 0x0c0c0100u) : 0x0c0c0c0cu;
    selO[k] = (k == (jj >> 1)) ? ((jj & 1) ? 0x03020c0cu : 0x0c0c0302u) : 0x0c0c0c0cu;
  }
  auto buildA = [&](unsigned pw, int odd) -> v4u {
    v4u a;
    if (odd) { a.x = __builtin_amdgcn_perm(pw, pw, selO[0]); a.y = __builtin_amdgcn_perm(pw, pw, selO[1]); a.z = __builtin_amdgcn_perm(pw, pw, selO[2]); a.w = __builtin_amdgcn_perm(pw, pw, selO[3]); }
    else     { a.x = __builtin_amdgcn_perm(pw, pw, selE[0]); a.y = __builtin_amdgcn_perm(pw, pw, selE[1]); a.z = __builtin_amdgcn_perm(pw, pw, selE[2]); a.w = __builtin_amdgcn_perm(pw, pw, selE[3]); }
    return a;
  };
  auto arrive = [&](int c) {
    FENCE();
    if (lane == 0) __hip_atomic_fetch_add((LAS unsigned*)(L + L_CNT + 4 * c), 1u, __ATOMIC_RELAXED, __HIP_MEMORY_SCOPE_WORKGROUP);
    FENCE();
  };
  auto await = [&](int c, unsigned n) {
    FENCE();
    for (int s = 0; s < LSPIN_CAP; ++s) {
      if (*(volatile LAS unsigned*)(L + L_CNT + 4 * c) >= n) break;
    }
    FENCE();
  };

  auto arrive_a = [&](int c) {
    FENCE();
    if (lane == 0) asm volatile("ds_add_u32 %0, %1" ::"v"((unsigned)(L_CNT + 4 * c)), "v"(1u) : "memory");
    FENCE();
  };
  auto await_a = [&](int c, unsigned n) {
    FENCE();
    for (int s = 0; s < LSPIN_CAP; ++s) {
      unsigned v;
      asm volatile("ds_read_b32 %0, %1\n\ts_waitcnt lgkmcnt(0)" : "=v"(v) : "v"((unsigned)(L_CNT + 4 * c)) : "memory");
      if ((unsigned)__builtin_amdgcn_readfirstlane((int)v) >= n) break;
    }
    FENCE();
  };
#define GATHER(N, v, base, stride, tagv) GATHERX(N, v, base, stride, tagv, , 0u)
#define GATHERX(N, v, base, stride, tagv, EXTRA_LOAD, EXTRA_BAD)                                                \
  {                                                                                                             \
    unsigned pend_ = (1u << (N)) - 1u;                                                                          \
    for (int spin_ = 0; spin_ < SPIN_CAP; ++spin_) {                                                            \
      asm volatile("" ::: "memory");                                                                            \
      v4u t_[N];                                                                                                \
      EXTRA_LOAD;                                                                                               \
      _Pragma("unroll") for (int i_ = 0; i_ < (N); ++i_)                                                        \
        t_[i_] = __builtin_amdgcn_raw_buffer_load_b128(rs, ((pend_ >> i_) & 1u) ? (base) + i_ * (stride) : OOB, 0, 16); \
      _Pragma("unroll") for (int i_ = 0; i_ < (N); ++i_) {                                                      \
        const bool p_ = (pend_ >> i_) & 1u;                                                                     \
        v[i_].x = p_ ? t_[i_].x : v[i_].x; v[i_].y = p_ ? t_[i_].y : v[i_].y;                                   \
        v[i_].z = p_ ? t_[i_].z : v[i_].z; v[i_].w = p_ ? t_[i_].w : v[i_].w;                                   \
        if (bad4(v[i_], tagv) == 0) pend_ &= ~(1u << i_);                                                       \
      }                                                                                                         \
      if (__all((pend_ | (EXTRA_BAD)) == 0)) break;                                                             \
      __builtin_amdgcn_s_sleep(1);                                                                              \
    }                                                                                                           \
  }

  if (wave < 4) {
    const int w = wave;
    __builtin_amdgcn_s_setprio(PRIO);
    unsigned tag, epoch, rot;
    {
      const int dq = j & 1, cq = j >> 1;
      const u16* wkb = wuk + ((size_t)(128 * cq + 32 * w + r) * 32 + h) * 128 + 64 * dq + 8 * g;
      v4u wk[4];
#pragma unroll
      for (int f = 0; f < 4; ++f) wk[f] = ldnt(wkb + (size_t)(f >> 1) * (16 * 4096) + 32 * (f & 1));
      const u16* xp = x + (r >> 3) * 512 + 128 * w + 32 * g;
      v2u xv[8];
#pragma unroll
      for (int k = 0; k < 8; ++k) xv[k] = *(const v2u*)(xp + 16 * (k >> 2) + ((4 * k + 4 * cq) & 15));
      const u16* wqb = wq + (size_t)(128 * w + 32 * g + 16 * (r >> 3)) * 4096 + h * 128 + 64 * dq + 8 * (r & 7);
      v4u wv[16];
#pragma unroll
      for (int i = 0; i < 16; ++i) wv[i] = ldnt(wqb + (size_t)((i + 4 * cq) & 15) * 4096);
      epoch = __builtin_amdgcn_raw_buffer_load_b32(rs, vzero + W_EPOCH, 0, 16);
      FENCE();
      __builtin_amdgcn_s_barrier();
      FENCE();
      f32x4 acc = {0.f, 0.f, 0.f, 0.f}, acc2 = {0.f, 0.f, 0.f, 0.f};
#pragma unroll
      for (int i = 0; i < 16; ++i) {
        acc = mfma(buildA(xv[i >> 2][(i >> 1) & 1], i & 1), wv[i], acc);
        acc2 = mfma(buildA(xv[4 + (i >> 2)][(i >> 1) & 1], i & 1), wv[i], acc2);
      }
#pragma unroll
      for (int e = 0; e < 4; ++e) { float v = (r < 8) ? acc[e] : acc2[e]; acc[e] = v + dpp_ror<8>(v); }
      STAMP(1);
      if (r < 8) {
        const unsigned h01 = pk_bf16(acc.x, acc.y), h23 = pk_bf16(acc.z, acc.w);
        const unsigned l01 = pk_bf16(acc.x - bf_lo(h01), acc.y - bf_hi(h01)), l23 = pk_bf16(acc.z - bf_lo(h23), acc.w - bf_hi(h23));
        const int b = g >> 1, d0 = 8 * r + 4 * (g & 1);
        *(LAS v2u*)(L + L_P + (b + 4 * w) * 160 + d0 * 2) = v2u{h01, h23};
        *(LAS v2u*)(L + L_P + (b + 2 + 4 * w) * 160 + d0 * 2) = v2u{l01, l23};
      }
      arrive(C_S1);
      await(C_S1, 4);
      const v4u qb0 = LD128(L_P + r * 160 + g * 16), qb1 = LD128(L_P + r * 160 + 64 + g * 16);
      f32x4 qa = {0.f, 0.f, 0.f, 0.f}, qc = {0.f, 0.f, 0.f, 0.f};
      qa = mfma(wk[0], qb0, qa); qc = mfma(wk[2], qb0, qc);
      qa = mfma(wk[1], qb1, qa); qc = mfma(wk[3], qb1, qc);
#pragma unroll
      for (int e = 0; e < 4; ++e) {
        float v = qa[e]; v += dpp_ror<8>(v); v += dpp_ror<4>(v); v += dpp_ror<2>(v);
        float u = qc[e]; u += dpp_ror<8>(u); u += dpp_ror<4>(u); u += dpp_ror<2>(u);
        qa[e] = ((r & 2) ? u : v) * (0.08838834764831845f * 1.4426950408889634f);
      }
      tag = (epoch + 1u) & TMASK;
      rot = (epoch % NROT) * W_REG;
      if (r < 4)
        __builtin_amdgcn_raw_buffer_store_b128(tag4(qa, tag), rs,
            rot + W_H2 + (((((r & 1) * 2 + (h >> 4)) * 2 + dq) * 16 + (h & 15)) * 512 + 128 * cq + 32 * w + 16 * (r >> 1) + 4 * g) * 4, 0, 16);
    }
    STAMP(2);
    await(GTRIG ? C_ISS : C_KV, 4);
    STAMP(3);
    v4u kf[32];
    {
      v4u v[16];
      const unsigned base = rot + W_H2 + (unsigned)(ab * 2 + hg) * 65536u + (unsigned)tid * 16u;
#pragma unroll
      for (int i = 0; i < 16; ++i) v[i] = __builtin_amdgcn_raw_buffer_load_b128(rs, base + i * 4096u, 0, 16);
      arrive(C_PUB);
      if (GTRIG) await(C_KV, 4);
#pragma unroll
      for (int ks = 0; ks < 16; ++ks) {
        kf[2 * ks] = LD128(L_KV + (32 * w + r) * PITCH + ks * 64 + g * 16);
        kf[2 * ks + 1] = LD128(L_KV + (32 * w + 16 + r) * PITCH + ks * 64 + g * 16);
      }
      FENCE();
      unsigned bad0 = 0;
#pragma unroll
      for (int i = 0; i < 16; ++i) bad0 |= bad4(v[i], tag);
      if (!__all(bad0 == 0)) {
        for (int spin = 0; spin < SPIN_CAP; ++spin) {
          asm volatile("" ::: "memory");
#pragma unroll
          for (int i = 0; i < 16; ++i) v[i] = __builtin_amdgcn_raw_buffer_load_b128(rs, base + i * 4096u, 0, 16);
          unsigned bad = 0;
#pragma unroll
          for (int i = 0; i < 16; ++i) bad |= bad4(v[i], tag);
          if (__all(bad == 0)) break;
        }
        FENCE();
#pragma unroll
        for (int ks = 0; ks < 16; ++ks) {
          kf[2 * ks] = LD128(L_KV + (32 * w + r) * PITCH + ks * 64 + g * 16);
          kf[2 * ks + 1] = LD128(L_KV + (32 * w + 16 + r) * PITCH + ks * 64 + g * 16);
        }
      }
#pragma unroll
      for (int i = 0; i < 8; ++i) {
        const int head = 2 * i + (tid >> 7), c = 4 * (tid & 127);
        *(LAS v2u*)(L + L_Q + head * PITCH + c * 2) =
            v2u{pk_bf16(untag(v[i].x) + untag(v[i + 8].x), untag(v[i].y) + untag(v[i + 8].y)), pk_bf16(untag(v[i].z) + untag(v[i + 8].z), untag(v[i].w) + untag(v[i + 8].w))};
      }
    }
    STAMP(11);
    arrive(C_S2);
    await(C_S2, 4);
    STAMP(4);
    {
      f32x4 s0 = {0.f, 0.f, 0.f, 0.f}, s1 = {0.f, 0.f, 0.f, 0.f};
      const int tq = r >> 2, tp = r & 3;
      v4u vf[32];
      {
        v4u qf[16];
#pragma unroll
        for (int ks = 0; ks < 16; ++ks) qf[ks] = LD128(L_Q + r * PITCH + ks * 64 + g * 16);
        FENCE();
#pragma unroll
        for (int ks = 0; ks < 16; ++ks) {
          s0 = mfma(kf[2 * ks], qf[ks], s0);
          s1 = mfma(kf[2 * ks + 1], qf[ks], s1);
#pragma unroll
          for (int e = 0; e < 2; ++e) {
            const int ct = ks >> 1, i = 2 * (ks & 1) + e;
            const int a0 = L_KV + (32 * i + 4 * g + tq) * PITCH + (128 * w + 16 * ct + 4 * tp) * 2;
            const v2u t0 = __builtin_bit_cast(v2u, __builtin_amdgcn_ds_read_tr16_b64_v4i16((LAS s16x4*)(L + a0)));
            const v2u t1 = __builtin_bit_cast(v2u, __builtin_amdgcn_ds_read_tr16_b64_v4i16((LAS s16x4*)(L + a0 + 16 * PITCH)));
            vf[4 * ct + i] = v4u{t0.x, t0.y, t1.x, t1.y};
          }
          __builtin_amdgcn_sched_barrier(0);
        }
      }
      FENCE();
      float mx = fmaxf(fmaxf(fmaxf(s0.x, s0.y), fmaxf(s0.z, s0.w)), fmaxf(fmaxf(s1.x, s1.y), fmaxf(s1.z, s1.w)));
      mx = x32_max(x16_max(mx));
      if (g == 0) *(LAS float*)(L + L_SM + (r * 4 + w) * 4) = mx;
      arrive(C_S3);
      await(C_S3, 4);
      const f32x4 m0 = LDF4(L_SM + r * 16);
      float M = fmaxf(fmaxf(m0.x, m0.y), fmaxf(m0.z, m0.w));
      f32x4 p0, p1;
#pragma unroll
      for (int e = 0; e < 4; ++e) { p0[e] = __builtin_amdgcn_exp2f(s0[e] - M); p1[e] = __builtin_amdgcn_exp2f(s1[e] - M); }
      *(LAS v4u*)(L + L_P + w * 1024 + lane * 16) = v4u{pk_bf16(p0.x, p0.y), pk_bf16(p0.z, p0.w), pk_bf16(p1.x, p1.y), pk_bf16(p1.z, p1.w)};
      float ls = ((p0.x + p0.y) + (p0.z + p0.w)) + ((p1.x + p1.y) + (p1.z + p1.w));
      ls = x32_add(x16_add(ls));
      if (g == 0) *(LAS float*)(L + L_SM + 256 + (r * 4 + w) * 4) = ls;
      arrive(C_S4);
      await(C_S4, 4);
      STAMP(5);
      v4u pf[4];
#pragma unroll
      for (int i = 0; i < 4; ++i) pf[i] = LD128(L_P + i * 1024 + lane * 16);
      unsigned lsb;
      float osc;
      {
        const f32x4 l0 = LDF4(L_SM + 256 + r * 16);
        const float lse = M + __builtin_amdgcn_logf((l0.x + l0.y) + (l0.z + l0.w));
        lsb = (__float_as_uint(lse) + 0x80u) & 0xffffff00u;
        osc = __builtin_amdgcn_exp2f(M - __uint_as_float(lsb));
      }
      const unsigned f1 = lsb >> 20, f3 = (lsb >> 8) & TMASK;
      const int stg = L_KV + w * (32 * 272);
#pragma unroll
      for (int ct = 0; ct < 8; ++ct) {
        f32x4 o = {0.f, 0.f, 0.f, 0.f};
#pragma unroll
        for (int i = 0; i < 4; ++i) o = mfma(vf[4 * ct + i], pf[i], o);
        o *= osc;
        *(LAS v4u*)(L + stg + ((ct >> 2) * 16 + r) * 272 + ((ct & 3) * 4 + g) * 16) = v4u{tagw(o.x, tag), tagw(o.y, f1), tagw(o.z, tag), tagw(o.w, f3)};
      }
      FENCE();
#pragma unroll
      for (int k = 0; k < 8; ++k) {
        const v4u d = LD128(stg + (4 * k + g) * 272 + r * 16);
        __builtin_amdgcn_raw_buffer_store_b128(d, rs, rot + W_H3 + (((ab * 64 + as) * 2 + hg) * 32 + 8 * w + k) * 1024 + lane * 16, 0, 16);
      }
    }
    arrive(C_ST);
    STAMP(6);
    await(C_KVH, 4);
    v4u wvv[4];
#pragma unroll
    for (int i = 0; i < 4; ++i) wvv[i] = LD128(L_WV + (w * 4 + i) * 1024 + lane * 16);
    __builtin_amdgcn_s_sleep(H3_DELAY);
    {
      const int b = w >> 1;
      const int c4 = tid & 15, sg = (tid >> 4) & 7;
      v4u v[8] = {};
      const unsigned obase = rot + W_H3 + (unsigned)(((b * 64 + sg) * 2 + (h >> 4)) * 32 + 4 * j) * 1024u + (h & 15) * 256u + c4 * 16u;
      {
        unsigned pend = 0xffu;
        for (int spin = 0; spin < SPIN_CAP; ++spin) {
          asm volatile("" ::: "memory");
          v4u t[8];
#pragma unroll
          for (int i = 0; i < 8; ++i) t[i] = __builtin_amdgcn_raw_buffer_load_b128(rs, ((pend >> i) & 1u) ? obase + i * (8u * 65536u) : OOB, 0, 16);
          unsigned badm = 0;
#pragma unroll
          for (int i = 0; i < 8; ++i) badm |= ((((t[i].x ^ tag) | (t[i].z ^ tag)) & TMASK) ? 1u : 0u) << i;
          badm |= (unsigned)__builtin_amdgcn_update_dpp(0, (int)badm, 0x128, 0xf, 0xf, false);
          badm |= (unsigned)__builtin_amdgcn_update_dpp(0, (int)badm, 0x124, 0xf, 0xf, false);
          badm |= (unsigned)__builtin_amdgcn_update_dpp(0, (int)badm, 0x122, 0xf, 0xf, false);
          badm |= (unsigned)__builtin_amdgcn_update_dpp(0, (int)badm, 0x121, 0xf, 0xf, false);
#pragma unroll
          for (int i = 0; i < 8; ++i) {
            const bool p = (pend >> i) & 1u;
            v[i].x = p ? t[i].x : v[i].x; v[i].y = p ? t[i].y : v[i].y; v[i].z = p ? t[i].z : v[i].z; v[i].w = p ? t[i].w : v[i].w;
          }
          pend &= badm;
          if (__all(pend == 0)) break;
          __builtin_amdgcn_s_sleep(H3_SLEEP);
        }
      }
      STAMP(7);
      float ls[8];
      float Mw = -3.0e38f;
#pragma unroll
      for (int i = 0; i < 8; ++i) { ls[i] = __uint_as_float(((v[i].y & TMASK) << 20) | ((v[i].w & TMASK) << 8)); Mw = fmaxf(Mw, ls[i]); }
      Mw = x32_max(x16_max(Mw));
      f32x4 o4 = {0.f, 0.f, 0.f, 0.f};
      float Lw = 0.f;
#pragma unroll
      for (int i = 0; i < 8; ++i) {
        const float ws_ = __builtin_amdgcn_exp2f(ls[i] - Mw);
        Lw += ws_;
        o4.x += ws_ * untag(v[i].x); o4.y += ws_ * untag(v[i].y); o4.z += ws_ * untag(v[i].z); o4.w += ws_ * untag(v[i].w);
      }
#pragma unroll
      for (int e2 = 0; e2 < 4; ++e2) o4[e2] = x32_add(x16_add(o4[e2]));
      Lw = x32_add(x16_add(Lw));
      if (g == 0) *(LAS f32x4*)(L + L_OL + w * 256 + c4 * 16) = o4;
      if (lane == 0) *(LAS v2u*)(L + L_SM + w * 8) = v2u{__float_as_uint(Mw), __float_as_uint(Lw)};
    }
    arrive(C_S5);
    await(C_S5, 4);
    {
      const int b = r >> 3;
      const f32x4 oa = LDF4(L_OL + (2 * b) * 256 + (16 * w + 4 * g) * 4), ob = LDF4(L_OL + (2 * b + 1) * 256 + (16 * w + 4 * g) * 4);
      const f32x4 ml = LDF4(L_SM + b * 16);
      const float Mb = fmaxf(ml.x, ml.z);
      const float ea = __builtin_amdgcn_exp2f(ml.x - Mb), eb = __builtin_amdgcn_exp2f(ml.z - Mb);
      const float inv = 1.f / (ml.y * ea + ml.w * eb);
      const f32x4 ov = oa * (ea * inv) + ob * (eb * inv);
      const unsigned h01 = pk_bf16(ov.x, ov.y), h23 = pk_bf16(ov.z, ov.w);
      const unsigned l01 = pk_bf16(ov.x - bf_lo(h01), ov.y - bf_hi(h01)), l23 = pk_bf16(ov.z - bf_lo(h23), ov.w - bf_hi(h23));
      f32x4 c0 = {0.f, 0.f, 0.f, 0.f}, c1 = {0.f, 0.f, 0.f, 0.f};
      c0 = mfma(buildA(h01, 0), wvv[0], c0); c1 = mfma(buildA(l01, 0), wvv[0], c1);
      c0 = mfma(buildA(h01, 1), wvv[1], c0); c1 = mfma(buildA(l01, 1), wvv[1], c1);
      c0 = mfma(buildA(h23, 0), wvv[2], c0); c1 = mfma(buildA(l23, 0), wvv[2], c1);
      c0 = mfma(buildA(h23, 1), wvv[3], c0); c1 = mfma(buildA(l23, 1), wvv[3], c1);
      *(LAS f32x4*)(L + L_CTX + w * 1024 + (g >> 1) * 512 + (8 * r + 4 * (g & 1)) * 4) = c0 + c1;
    }
    arrive(C_CTX);
    STAMP(8);
    if (w == 0) {
      v4u q[4] = {};
      const unsigned qbase = rot + W_H4 + (unsigned)lane * 4096u + bid * 16u;
      __builtin_amdgcn_s_sleep(H4_DELAY);
      GATHER(4, q, qbase, 64u * 4096u, tag);
      STAMP(9);
      f32x4 t4;
#pragma unroll
      for (int e2 = 0; e2 < 4; ++e2) t4[e2] = wave_sum((untag(q[0][e2]) + untag(q[1][e2])) + (untag(q[2][e2]) + untag(q[3][e2])));
      float dval = t4.x;
      if (bid == 0) {
        float mn = 1e30f, mx = -1e30f;
#pragma unroll
        for (int k = 0; k < 4; ++k) {
          const v2u t = __builtin_amdgcn_raw_buffer_load_b64(rs, W_DBG + (k * 64 + lane) * 8, 0, 16);
          const float d = (float)(int)(t.x - (unsigned)tstart_) * 0.01f;
          mn = fminf(mn, d); mx = fmaxf(mx, d);
        }
        mx = wave_max(mx); mn = -wave_max(-mn);
        const float own = (float)(__builtin_amdgcn_s_memrealtime() - tstart_) * 0.01f;
        if (DIAG == 0) { if (own - mn > TG) dval = 4.f * (own - mn); } else if (DIAG == 1) { if (mx - mn > TG) dval = 100.f + 8.f * (mx - mn); } else { if (-mn > TG) dval = 100.f + 8.f * (-mn); }
      }
      if (lane == 0) {
        *(v2u*)(out + bid * 4) = v2u{pk_bf16(bid == 0 ? dval : t4.x, t4.y), pk_bf16(t4.z, t4.w)};
        if (bid == 0) __builtin_amdgcn_raw_buffer_store_b32(epoch + 1u, rs, W_EPOCH, 0, 16);
      }
    }
    STAMP(10);
    STAMP_FLUSH();
  } else {
    const int w = wave - 4;
    if (w == 0 && lane < 16) *(LAS unsigned*)(L + L_CNT + 4 * lane) = 0u;
    asm volatile("s_waitcnt lgkmcnt(0)" ::: "memory");
    FENCE();
    __builtin_amdgcn_s_barrier();
    FENCE();
    v4u wov[34];
    const u16* wob = wo + ((size_t)h * 128 + 32 * g) * 512 + 128 * w + 8 * r;
#pragma unroll
    for (int i = 0; i < 4; ++i) wov[i] = ldnt(wob + ((i + 4 * j) & 31) * 512);
    const v4u gx0 = *(const v4u*)(x + lane * 8), gx1 = *(const v4u*)(x + 512 + lane * 8), gwv = *(const v4u*)(gw + lane * 8);
    const unsigned epoch = __builtin_amdgcn_raw_buffer_load_b32(rs, vzero + W_EPOCH, 0, 16);
    v4u fv[8]; v4u fx;
    const bool ffn1 = (w < 2) && (bid < 224);
    const int cb = bid % 14, rb = bid / 14;
    if (ffn1) {
      const u16* fb = (w == 0 ? wg : wu) + (size_t)(32 * rb + 8 * g) * 1792 + 128 * cb + 8 * r;
#pragma unroll
      for (int i = 0; i < 8; ++i) fv[i] = ldnt(fb + (size_t)i * 1792);
      fx = *(const v4u*)(x + (r >> 3) * 512 + 32 * rb + 8 * g);
    }
    {
      const int u = bid < 224 ? bid : 0;
      const u16* wdb = wd + (size_t)(8 * u + 2 * g) * 512 + 128 * w + 8 * r;
      wov[32] = ldnt(wdb);
      wov[33] = ldnt(wdb + 512);
    }
    v4u wvl[4];
    {
      const u16* wvb = wuv + ((size_t)(64 * j + 16 * w + 4 * g) * 32 + h) * 128 + 8 * r;
#pragma unroll
      for (int i = 0; i < 4; ++i) wvl[i] = ldnt(wvb + (size_t)i * 4096);
    }
    FENCE();
    const u16* src = ckv + ((size_t)ab * 8192 + as * 128 + w * 32) * 512 + lane * 8;
#pragma unroll
    for (int i = 0; i < 32; ++i)
      __builtin_amdgcn_global_load_lds((const void*)(src + i * 512), (LAS void*)(L + L_KV + (w * 32 + i) * PITCH), 16, 0, 2);
    arrive_a(C_ISS);
    __builtin_amdgcn_s_waitcnt(0x0f70);
    arrive_a(C_KV);
    STAMP(1);
    const unsigned tag = (epoch + 1u) & TMASK;
    const unsigned rot = (epoch % NROT) * W_REG;
    if (ffn1) {
      f32x4 acc = {0.f, 0.f, 0.f, 0.f};
#pragma unroll
      for (int i = 0; i < 8; ++i) acc = mfma(buildA(fx[i >> 1], i & 1), fv[i], acc);
      const unsigned u = 16 * cb + r;
      __builtin_amdgcn_raw_buffer_store_b128(tag4(acc, tag), rs, rot + W_HF + u * 2048 + ((w * 16 + rb) * 4 + g) * 16, 0, 16);
    }
    float gate0, gate1;
    {
      float s0 = 0.f, s1 = 0.f;
#pragma unroll
      for (int k = 0; k < 4; ++k) {
        s0 += bf_lo(gx0[k]) * bf_lo(gwv[k]) + bf_hi(gx0[k]) * bf_hi(gwv[k]);
        s1 += bf_lo(gx1[k]) * bf_lo(gwv[k]) + bf_hi(gx1[k]) * bf_hi(gwv[k]);
      }
      s0 = wave_sum(s0); s1 = wave_sum(s1);
      gate0 = 1.f / (1.f + __expf(-s0));
      gate1 = 1.f / (1.f + __expf(-s1));
    }
    STAMP(2);
    await_a(C_PUB, 4);
#pragma unroll
    for (int i = 4; i < 32; ++i) wov[i] = ldnt(wob + ((i + 4 * j) & 31) * 512);
    STAMP(4);
    await_a(C_S4, 4);
#pragma unroll
    for (int i = 0; i < 4; ++i) *(LAS v4u*)(L + L_WV + (w * 4 + i) * 1024 + lane * 16) = wvl[i];
    arrive_a(C_KVH);
    STAMP(6);
    unsigned hp = 0u, lp = 0u;
    if (bid < 224) {
      v4u ff[2] = {};
      const unsigned fbase = rot + W_HF + bid * 2048 + lane * 16;
      GATHER(2, ff, fbase, 1024u, tag);
      const v4u f0 = ff[0], f1 = ff[1];
      f32x4 hv;
      const float og = 1.f - (((lane >> 1) & 1) ? gate1 : gate0);
#pragma unroll
      for (int e2 = 0; e2 < 4; ++e2) {
        float t = untag(f0[e2]); t += dpp_ror<8>(t); t += dpp_ror<4>(t); t = x32_add(x16_add(t));
        float u = untag(f1[e2]); u += dpp_ror<8>(u); u += dpp_ror<4>(u); u = x32_add(x16_add(u));
        const float a2 = 2.f * 0.7978845608028654f * (t + 0.044715f * t * t * t);
        hv[e2] = t / (1.f + __expf(-a2)) * u * og;
      }
      const int srcl = (r >> 3) * 2 + (g >> 1);
      const float a0 = __shfl(hv.x, srcl), a1 = __shfl(hv.y, srcl), a2_ = __shfl(hv.z, srcl), a3 = __shfl(hv.w, srcl);
      const float h0 = (g & 1) ? a2_ : a0, h1 = (g & 1) ? a3 : a1;
      hp = pk_bf16(h0, h1);
      lp = pk_bf16(h0 - bf_lo(hp), h1 - bf_hi(hp));
    }
    f32x4 acc = {0.f, 0.f, 0.f, 0.f};
    acc = mfma(buildA(hp, 0), wov[32], acc);
    acc = mfma(buildA(hp, 1), wov[33], acc);
    acc = mfma(buildA(lp, 0), wov[32], acc);
    acc = mfma(buildA(lp, 1), wov[33], acc);
    STAMP(7);
    await_a(C_CTX, 4);
    STAMP(8);
    {
      const int b = r >> 3;
      const float gb = b ? gate1 : gate0;
      const int off = L_CTX + b * 512 + (32 * g + 4 * jj) * 4;
      const f32x4 cs = ((LDF4(off) + LDF4(off + 1024)) + (LDF4(off + 2048) + LDF4(off + 3072))) * gb;
      *(LAS v2u*)(L + L_PK + w * 512 + b * 256 + (16 * g + 2 * jj) * 4) = v2u{pk_bf16(cs.x, cs.y), pk_bf16(cs.z, cs.w)};
      FENCE();
      v2u pk[8];
#pragma unroll
      for (int k = 0; k < 8; ++k) pk[k] = *(const LAS v2u*)(L + L_PK + w * 512 + b * 256 + (16 * g + ((2 * k + 2 * j) & 15)) * 4);
#pragma unroll
      for (int i = 0; i < 32; ++i) acc = mfma(buildA(pk[i >> 2][(i >> 1) & 1], i & 1), wov[i], acc);
    }
    const unsigned cons = (g >> 1) * 128 + 32 * w + 2 * r + (g & 1);
    __builtin_amdgcn_raw_buffer_store_b128(tag4(acc, tag), rs, rot + W_H4 + bid * 4096 + cons * 16, 0, 16);
    STAMP(9);
    STAMP_FLUSH();
  }
}
}

extern "C" void kernel_launch(void* const* d_in, const int* in_sizes, int n_in, void* d_out, int out_size, void* d_ws, size_t ws_size,
                              hipStream_t stream) {
  (void)in_sizes; (void)n_in; (void)out_size; (void)ws_size;
  (void)hipFuncSetAttribute((const void*)mk, hipFuncAttributeMaxDynamicSharedMemorySize, L_ALLOC);
  mk<<<256, 512, L_ALLOC, stream>>>((const u16*)d_in[0], (const u16*)d_in[1], (const u16*)d_in[2], (const u16*)d_in[3], (const u16*)d_in[4],
                                  (const u16*)d_in[5], (const u16*)d_in[7], (const u16*)d_in[8], (const u16*)d_in[9], (const u16*)d_in[10],
                                  (u16*)d_out, (unsigned char*)d_ws);
}
```

```cpp
#include <hip/hip_runtime.h>
#include <hip/hip_bf16.h>
#include <stdint.h>

typedef unsigned short u16;
typedef unsigned v4u __attribute__((ext_vector_type(4)));
typedef unsigned v2u __attribute__((ext_vector_type(2)));
typedef float f32x4 __attribute__((ext_vector_type(4)));
typedef short s16x4 __attribute__((ext_vector_type(4)));
typedef __bf16 bf16x8 __attribute__((ext_vector_type(8)));
#define LAS __attribute__((address_space(3)))
#define GAS __attribute__((address_space(1)))

namespace {
constexpr int PITCH = 1056;
constexpr int L_KV = 0;
constexpr int L_Q = 128 * PITCH;
constexpr int L_P = L_Q + 16 * PITCH;
constexpr int L_SM = L_P + 4608;
constexpr int L_OL = L_Q;
constexpr int L_CTX = L_OL + 1024;
constexpr int L_PK = L_CTX + 4096;
constexpr int L_CNT = L_SM + 512;
constexpr int L_END = L_CNT + 64;
static_assert(L_END <= 160 * 1024, "LDS");
enum { C_S1 = 0, C_S2, C_KV, C_S3, C_S4, C_S5, C_CTX, C_ST, C_FIN, C_KVH, C_PUB, C_ISS };
constexpr unsigned OOB = 0xfffffff0u;

constexpr unsigned W_EPOCH = 0;
constexpr unsigned W_H2 = 4096;
constexpr unsigned W_HF = W_H2 + 2 * 2 * 32 * 512 * 4;
constexpr unsigned W_H4 = W_HF + 224 * 2048;
constexpr unsigned W_H3 = W_H4 + 256 * 256 * 16;
constexpr unsigned W_DBG = W_H3 + 32 * 8 * 2 * 64 * 64 * 4;
constexpr unsigned W_END = W_DBG + 256 * 2 * 16 * 8;
#define H3_SLEEP 8
#define H2_DELAY 20
#define H3_GATE 1
#define ISS_AT 20
#define TLBW 1
#define DMA0 8
#define WO_B 32
#define PRIO 3
#define HOLD2 0
#define KVH_N 20
#define WO_A 4
constexpr int SPIN_CAP = 1 << 15;
constexpr int LSPIN_CAP = 1 << 22;

__device__ __forceinline__ f32x4 mfma(v4u a, v4u b, f32x4 c) {
  return __builtin_amdgcn_mfma_f32_16x16x32_bf16(__builtin_bit_cast(bf16x8, a), __builtin_bit_cast(bf16x8, b), c, 0, 0, 0);
}
__device__ __forceinline__ float bf_lo(unsigned w) { return __uint_as_float(w << 16); }
__device__ __forceinline__ float bf_hi(unsigned w) { return __uint_as_float(w & 0xffff0000u); }
__device__ __forceinline__ unsigned pk_bf16(float a, float b) {
  typedef __bf16 bf16x2 __attribute__((ext_vector_type(2)));
  typedef float f32x2 __attribute__((ext_vector_type(2)));
  f32x2 v = {a, b};
  bf16x2 r = __builtin_convertvector(v, bf16x2);
  return __builtin_bit_cast(unsigned, r);
}
constexpr unsigned TMASK = 0xfffu;
__device__ __forceinline__ unsigned tagw(float v, unsigned tag) { return ((__float_as_uint(v) + 0x800u) & ~TMASK) | tag; }
__device__ __forceinline__ float untag(unsigned w) { return __uint_as_float(w & ~TMASK); }
__device__ __forceinline__ v4u tag4(f32x4 v, unsigned tag) { return v4u{tagw(v.x, tag), tagw(v.y, tag), tagw(v.z, tag), tagw(v.w, tag)}; }
__device__ __forceinline__ unsigned bad4(v4u w, unsigned tag) { return ((w.x ^ tag) | (w.y ^ tag) | (w.z ^ tag) | (w.w ^ tag)) & TMASK; }
template <int N> __device__ __forceinline__ float dpp_ror(float v) {
  return __int_as_float(__builtin_amdgcn_update_dpp(0, __float_as_int(v), 0x120 + N, 0xf, 0xf, false));
}
__device__ __forceinline__ float x16_add(float v) { auto p = __builtin_amdgcn_permlane16_swap(__float_as_uint(v), __float_as_uint(v), false, false); return __uint_as_float(p[0]) + __uint_as_float(p[1]); }
__device__ __forceinline__ float x32_add(float v) { auto p = __builtin_amdgcn_permlane32_swap(__float_as_uint(v), __float_as_uint(v), false, false); return __uint_as_float(p[0]) + __uint_as_float(p[1]); }
__device__ __forceinline__ float x16_max(float v) { auto p = __builtin_amdgcn_permlane16_swap(__float_as_uint(v), __float_as_uint(v), false, false); return fmaxf(__uint_as_float(p[0]), __uint_as_float(p[1])); }
__device__ __forceinline__ float x32_max(float v) { auto p = __builtin_amdgcn_permlane32_swap(__float_as_uint(v), __float_as_uint(v), false, false); return fmaxf(__uint_as_float(p[0]), __uint_as_float(p[1])); }
__device__ __forceinline__ float wave_sum(float v) { v += dpp_ror<8>(v); v += dpp_ror<4>(v); v += dpp_ror<2>(v); v += dpp_ror<1>(v); v = x16_add(v); return x32_add(v); }
__device__ __forceinline__ float wave_max(float v) { v = fmaxf(v, dpp_ror<8>(v)); v = fmaxf(v, dpp_ror<4>(v)); v = fmaxf(v, dpp_ror<2>(v)); v = fmaxf(v, dpp_ror<1>(v)); v = x16_max(v); return x32_max(v); }
__device__ __forceinline__ v4u ldnt(const u16* p) { return *(const v4u*)p; }

#define LD128(off) (*(const LAS v4u*)(L + (off)))
#define LDF4(off) (*(const LAS f32x4*)(L + (off)))
#define FENCE() do { __builtin_amdgcn_sched_barrier(0); asm volatile("" ::: "memory"); } while (0)

#define STAMP(i)
#define STAMP_FLUSH()
#define L_ALLOC L_END

__global__ __launch_bounds__(512, 2) void mk(const u16* __restrict__ x, const u16* __restrict__ ckv, const u16* __restrict__ wq,
                                             const u16* __restrict__ wuk, const u16* __restrict__ wuv, const u16* __restrict__ wo,
                                             const u16* __restrict__ wg, const u16* __restrict__ wu, const u16* __restrict__ wd,
                                             const u16* __restrict__ gw, u16* __restrict__ out, unsigned char* ws) {
  extern __shared__ __attribute__((aligned(16))) unsigned char lds[];
  LAS unsigned char* L = (LAS unsigned char*)lds;
  const int tid = threadIdx.x, lane = tid & 63, wave = __builtin_amdgcn_readfirstlane(tid >> 6);
  const int bid = blockIdx.x, xcd = bid & 7, slot = bid >> 3;
  const int h = xcd * 4 + (slot >> 3), j = slot & 7;
  const int ab = xcd >> 2, as = (xcd & 3) * 16 + (slot >> 1), hg = slot & 1;
  const int g = lane >> 4, r = lane & 15, jj = r & 7;
  const auto rs = __builtin_amdgcn_make_buffer_rsrc(ws, 0, W_END, 0x00020000);
  STAMP(0);
  int vzero = 0;
  asm volatile("" : "+v"(vzero));
  unsigned selE[4], selO[4];
#pragma unroll
  for (int k = 0; k < 4; ++k) {
    selE[k] = (k == (jj >> 1)) ? ((jj & 1) ? 0x01000c0cu : 0x0c0c0100u) : 0x0c0c0c0cu;
    selO[k] = (k == (jj >> 1)) ? ((jj & 1) ? 0x03020c0cu : 0x0c0c0302u) : 0x0c0c0c0cu;
  }
  auto buildA = [&](unsigned pw, int odd) -> v4u {
    v4u a;
    if (odd) { a.x = __builtin_amdgcn_perm(pw, pw, selO[0]); a.y = __builtin_amdgcn_perm(pw, pw, selO[1]); a.z = __builtin_amdgcn_perm(pw, pw, selO[2]); a.w = __builtin_amdgcn_perm(pw, pw, selO[3]); }
    else     { a.x = __builtin_amdgcn_perm(pw, pw, selE[0]); a.y = __builtin_amdgcn_perm(pw, pw, selE[1]); a.z = __builtin_amdgcn_perm(pw, pw, selE[2]); a.w = __builtin_amdgcn_perm(pw, pw, selE[3]); }
    return a;
  };
  auto arrive = [&](int c) {
    FENCE();
    if (lane == 0) __hip_atomic_fetch_add((LAS unsigned*)(L + L_CNT + 4 * c), 1u, __ATOMIC_RELAXED, __HIP_MEMORY_SCOPE_WORKGROUP);
    FENCE();
  };
  auto await = [&](int c, unsigned n) {
    FENCE();
    for (int s = 0; s < LSPIN_CAP; ++s) {
      if (*(volatile LAS unsigned*)(L + L_CNT + 4 * c) >= n) break;
    }
    FENCE();
  };

  auto arrive_a = [&](int c) {
    FENCE();
    if (lane == 0) asm volatile("ds_add_u32 %0, %1" ::"v"((unsigned)(L_CNT + 4 * c)), "v"(1u) : "memory");
    FENCE();
  };
  auto await_a = [&](int c, unsigned n) {
    FENCE();
    for (int s = 0; s < LSPIN_CAP; ++s) {
      unsigned v;
      asm volatile("ds_read_b32 %0, %1\n\ts_waitcnt lgkmcnt(0)" : "=v"(v) : "v"((unsigned)(L_CNT + 4 * c)) : "memory");
      if ((unsigned)__builtin_amdgcn_readfirstlane((int)v) >= n) break;
    }
    FENCE();
  };
#define GATHER(N, v, base, stride, tagv) GATHERX(N, v, base, stride, tagv, , 0u)
#define GATHERX(N, v, base, stride, tagv, EXTRA_LOAD, EXTRA_BAD)                                                \
  {                                                                                                             \
    unsigned pend_ = (1u << (N)) - 1u;                                                                          \
    for (int spin_ = 0; spin_ < SPIN_CAP; ++spin_) {                                                            \
      asm volatile("" ::: "memory");                                                                            \
      v4u t_[N];                                                                                                \
      EXTRA_LOAD;                                                                                               \
      _Pragma("unroll") for (int i_ = 0; i_ < (N); ++i_)                                                        \
        t_[i_] = __builtin_amdgcn_raw_buffer_load_b128(rs, ((pend_ >> i_) & 1u) ? (base) + i_ * (stride) : OOB, 0, 16); \
      _Pragma("unroll") for (int i_ = 0; i_ < (N); ++i_) {                                                      \
        const bool p_ = (pend_ >> i_) & 1u;                                                                     \
        v[i_].x = p_ ? t_[i_].x : v[i_].x; v[i_].y = p_ ? t_[i_].y : v[i_].y;                                   \
        v[i_].z = p_ ? t_[i_].z : v[i_].z; v[i_].w = p_ ? t_[i_].w : v[i_].w;                                   \
        if (bad4(v[i_], tagv) == 0) pend_ &= ~(1u << i_);                                                       \
      }                                                                                                         \
      if (__all((pend_ | (EXTRA_BAD)) == 0)) break;                                                             \
      __builtin_amdgcn_s_sleep(1);                                                                              \
    }                                                                                                           \
  }

  if (wave < 4) {
    const int w = wave;
    __builtin_amdgcn_s_setprio(PRIO);
    unsigned tag, epoch;
    {
      const int dq = j & 1, cq = j >> 1;
      const u16* wkb = wuk + ((size_t)(128 * cq + 32 * w + r) * 32 + h) * 128 + 64 * dq + 8 * g;
      v4u wk[4];
#pragma unroll
      for (int f = 0; f < 4; ++f) wk[f] = ldnt(wkb + (size_t)(f >> 1) * (16 * 4096) + 32 * (f & 1));
      const u16* xp = x + (r >> 3) * 512 + 128 * w + 32 * g;
      v2u xv[8];
#pragma unroll
      for (int k = 0; k < 8; ++k) xv[k] = *(const v2u*)(xp + 16 * (k >> 2) + ((4 * k + 4 * cq) & 15));
      const u16* wqb = wq + (size_t)(128 * w + 32 * g + 16 * (r >> 3)) * 4096 + h * 128 + 64 * dq + 8 * (r & 7);
      v4u wv[16];
#pragma unroll
      for (int i = 0; i < 16; ++i) wv[i] = ldnt(wqb + (size_t)((i + 4 * cq) & 15) * 4096);
      epoch = __builtin_amdgcn_raw_buffer_load_b32(rs, vzero + W_EPOCH, 0, 16);
      FENCE();
      __builtin_amdgcn_s_barrier();
      FENCE();
      f32x4 acc = {0.f, 0.f, 0.f, 0.f}, acc2 = {0.f, 0.f, 0.f, 0.f};
#pragma unroll
      for (int i = 0; i < 16; ++i) {
        acc = mfma(buildA(xv[i >> 2][(i >> 1) & 1], i & 1), wv[i], acc);
        acc2 = mfma(buildA(xv[4 + (i >> 2)][(i >> 1) & 1], i & 1), wv[i], acc2);
      }
#pragma unroll
      for (int e = 0; e < 4; ++e) { float v = (r < 8) ? acc[e] : acc2[e]; acc[e] = v + dpp_ror<8>(v); }
      STAMP(1);
      if (r < 8) {
        const unsigned h01 = pk_bf16(acc.x, acc.y), h23 = pk_bf16(acc.z, acc.w);
        const unsigned l01 = pk_bf16(acc.x - bf_lo(h01), acc.y - bf_hi(h01)), l23 = pk_bf16(acc.z - bf_lo(h23), acc.w - bf_hi(h23));
        const int b = g >> 1, d0 = 8 * r + 4 * (g & 1);
        *(LAS v2u*)(L + L_P + (b + 4 * w) * 160 + d0 * 2) = v2u{h01, h23};
        *(LAS v2u*)(L + L_P + (b + 2 + 4 * w) * 160 + d0 * 2) = v2u{l01, l23};
      }
      arrive(C_S1);
      await(C_S1, 4);
      const v4u qb0 = LD128(L_P + r * 160 + g * 16), qb1 = LD128(L_P + r * 160 + 64 + g * 16);
      f32x4 qa = {0.f, 0.f, 0.f, 0.f}, qc = {0.f, 0.f, 0.f, 0.f};
      qa = mfma(wk[0], qb0, qa); qc = mfma(wk[2], qb0, qc);
      qa = mfma(wk[1], qb1, qa); qc = mfma(wk[3], qb1, qc);
#pragma unroll
      for (int e = 0; e < 4; ++e) {
        float v = qa[e]; v += dpp_ror<8>(v); v += dpp_ror<4>(v); v += dpp_ror<2>(v);
        float u = qc[e]; u += dpp_ror<8>(u); u += dpp_ror<4>(u); u += dpp_ror<2>(u);
        qa[e] = ((r & 2) ? u : v) * (0.08838834764831845f * 1.4426950408889634f);
      }
      tag = (epoch + 1u) & TMASK;
      if (r < 4)
        __builtin_amdgcn_raw_buffer_store_b128(tag4(qa, tag), rs,
            W_H2 + (((((r & 1) * 2 + (h >> 4)) * 2 + dq) * 16 + (h & 15)) * 512 + 128 * cq + 32 * w + 16 * (r >> 1) + 4 * g) * 4, 0, 16);
    }
    STAMP(2);
    await(C_ISS, 4);
    STAMP(3);
    v4u kf[32];
    {
      v4u v[16];
      const unsigned base = W_H2 + (unsigned)(ab * 2 + hg) * 65536u + (unsigned)tid * 16u;
#pragma unroll
      for (int i = 0; i < 16; ++i) v[i] = __builtin_amdgcn_raw_buffer_load_b128(rs, base + i * 4096u, 0, 16);
      await(C_KV, 4);
      STAMP(12);
#pragma unroll
      for (int ks = 0; ks < 16; ++ks) {
        kf[2 * ks] = LD128(L_KV + (32 * w + r) * PITCH + ks * 64 + g * 16);
        kf[2 * ks + 1] = LD128(L_KV + (32 * w + 16 + r) * PITCH + ks * 64 + g * 16);
      }
      FENCE();
      unsigned bad0 = 0;
#pragma unroll
      for (int i = 0; i < 16; ++i) bad0 |= bad4(v[i], tag);
      if (!__all(bad0 == 0)) {
        for (int spin = 0; spin < SPIN_CAP; ++spin) {
          asm volatile("" ::: "memory");
#pragma unroll
          for (int i = 0; i < 16; ++i) v[i] = __builtin_amdgcn_raw_buffer_load_b128(rs, base + i * 4096u, 0, 16);
          unsigned bad = 0;
#pragma unroll
          for (int i = 0; i < 16; ++i) bad |= bad4(v[i], tag);
          if (__all(bad == 0)) break;
        }
        FENCE();
#pragma unroll
        for (int ks = 0; ks < 16; ++ks) {
          kf[2 * ks] = LD128(L_KV + (32 * w + r) * PITCH + ks * 64 + g * 16);
          kf[2 * ks + 1] = LD128(L_KV + (32 * w + 16 + r) * PITCH + ks * 64 + g * 16);
        }
      }
#pragma unroll
      for (int i = 0; i < 8; ++i) {
        const int head = 2 * i + (tid >> 7), c = 4 * (tid & 127);
        *(LAS v2u*)(L + L_Q + head * PITCH + c * 2) =
            v2u{pk_bf16(untag(v[i].x) + untag(v[i + 8].x), untag(v[i].y) + untag(v[i + 8].y)), pk_bf16(untag(v[i].z) + untag(v[i + 8].z), untag(v[i].w) + untag(v[i + 8].w))};
      }
    }
    STAMP(11);
    arrive(C_S2);
    v4u wvv[4];
    {
      const u16* wvb = wuv + ((size_t)(64 * j + 16 * w + 4 * g) * 32 + h) * 128 + 8 * r;
#pragma unroll
      for (int i = 0; i < 4; ++i) wvv[i] = ldnt(wvb + (size_t)i * 4096);
    }
    await(C_S2, 4);
    STAMP(4);
    {
      f32x4 s0 = {0.f, 0.f, 0.f, 0.f}, s1 = {0.f, 0.f, 0.f, 0.f};
      const int tq = r >> 2, tp = r & 3;
      v4u vf[32];
      {
        v4u qf[16];
#pragma unroll
        for (int ks = 0; ks < 16; ++ks) qf[ks] = LD128(L_Q + r * PITCH + ks * 64 + g * 16);
        FENCE();
#pragma unroll
        for (int ks = 0; ks < 16; ++ks) {
          s0 = mfma(kf[2 * ks], qf[ks], s0);
          s1 = mfma(kf[2 * ks + 1], qf[ks], s1);
#pragma unroll
          for (int e = 0; e < 2; ++e) {
            const int ct = ks >> 1, i = 2 * (ks & 1) + e;
            const int a0 = L_KV + (32 * i + 4 * g + tq) * PITCH + (128 * w + 16 * ct + 4 * tp) * 2;
            const v2u t0 = __builtin_bit_cast(v2u, __builtin_amdgcn_ds_read_tr16_b64_v4i16((LAS s16x4*)(L + a0)));
            const v2u t1 = __builtin_bit_cast(v2u, __builtin_amdgcn_ds_read_tr16_b64_v4i16((LAS s16x4*)(L + a0 + 16 * PITCH)));
            vf[4 * ct + i] = v4u{t0.x, t0.y, t1.x, t1.y};
          }
          __builtin_amdgcn_sched_barrier(0);
        }
      }
      FENCE();
      float mx = fmaxf(fmaxf(fmaxf(s0.x, s0.y), fmaxf(s0.z, s0.w)), fmaxf(fmaxf(s1.x, s1.y), fmaxf(s1.z, s1.w)));
      mx = x32_max(x16_max(mx));
      if (g == 0) *(LAS float*)(L + L_SM + (r * 4 + w) * 4) = mx;
      arrive(C_S3);
      await(C_S3, 4);
      const f32x4 m0 = LDF4(L_SM + r * 16);
      float M = fmaxf(fmaxf(m0.x, m0.y), fmaxf(m0.z, m0.w));
      f32x4 p0, p1;
#pragma unroll
      for (int e = 0; e < 4; ++e) { p0[e] = __builtin_amdgcn_exp2f(s0[e] - M); p1[e] = __builtin_amdgcn_exp2f(s1[e] - M); }
      *(LAS v4u*)(L + L_P + w * 1024 + lane * 16) = v4u{pk_bf16(p0.x, p0.y), pk_bf16(p0.z, p0.w), pk_bf16(p1.x, p1.y), pk_bf16(p1.z, p1.w)};
      float ls = ((p0.x + p0.y) + (p0.z + p0.w)) + ((p1.x + p1.y) + (p1.z + p1.w));
      ls = x32_add(x16_add(ls));
      if (g == 0) *(LAS float*)(L + L_SM + 256 + (r * 4 + w) * 4) = ls;
      arrive(C_S4);
      await(C_S4, 4);
      STAMP(5);
      v4u pf[4];
#pragma unroll
      for (int i = 0; i < 4; ++i) pf[i] = LD128(L_P + i * 1024 + lane * 16);
      unsigned lsb;
      float osc;
      {
        const f32x4 l0 = LDF4(L_SM + 256 + r * 16);
        const float lse = M + __builtin_amdgcn_logf((l0.x + l0.y) + (l0.z + l0.w));
        lsb = (__float_as_uint(lse) + 0x80u) & 0xffffff00u;
        osc = __builtin_amdgcn_exp2f(M - __uint_as_float(lsb));
      }
      const unsigned f1 = lsb >> 20, f3 = (lsb >> 8) & TMASK;
      const int stg = L_KV + w * (32 * 272);
#pragma unroll
      for (int ct = 0; ct < 8; ++ct) {
        f32x4 o = {0.f, 0.f, 0.f, 0.f};
#pragma unroll
        for (int i = 0; i < 4; ++i) o = mfma(vf[4 * ct + i], pf[i], o);
        o *= osc;
        *(LAS v4u*)(L + stg + ((ct >> 2) * 16 + r) * 272 + ((ct & 3) * 4 + g) * 16) = v4u{tagw(o.x, tag), tagw(o.y, f1), tagw(o.z, tag), tagw(o.w, f3)};
      }
      FENCE();
#pragma unroll
      for (int k = 0; k < 8; ++k) {
        const v4u d = LD128(stg + (4 * k + g) * 272 + r * 16);
        __builtin_amdgcn_raw_buffer_store_b128(d, rs, W_H3 + (((ab * 64 + as) * 2 + hg) * 32 + 8 * w + k) * 1024 + lane * 16, 0, 16);
      }
    }
    arrive(C_ST);
    STAMP(6);
    await(C_KVH, 4);
    {
      const int b = w >> 1;
      const int c4 = tid & 15, sg = (tid >> 4) & 7;
      v4u v[8] = {};
      const unsigned obase = W_H3 + (unsigned)(((b * 64 + sg) * 2 + (h >> 4)) * 32 + 4 * j) * 1024u + (h & 15) * 256u + c4 * 16u;
      {
        unsigned pend = 0xffu;
        for (int spin = 0; spin < SPIN_CAP; ++spin) {
          asm volatile("" ::: "memory");
          v4u t[8];
#pragma unroll
          for (int i = 0; i < 8; ++i) t[i] = __builtin_amdgcn_raw_buffer_load_b128(rs, ((pend >> i) & 1u) ? obase + i * (8u * 65536u) : OOB, 0, 16);
          unsigned badm = 0;
#pragma unroll
          for (int i = 0; i < 8; ++i) badm |= ((((t[i].x ^ tag) | (t[i].z ^ tag)) & TMASK) ? 1u : 0u) << i;
          badm |= (unsigned)__builtin_amdgcn_update_dpp(0, (int)badm, 0x128, 0xf, 0xf, false);
          badm |= (unsigned)__builtin_amdgcn_update_dpp(0, (int)badm, 0x124, 0xf, 0xf, false);
          badm |= (unsigned)__builtin_amdgcn_update_dpp(0, (int)badm, 0x122, 0xf, 0xf, false);
          badm |= (unsigned)__builtin_amdgcn_update_dpp(0, (int)badm, 0x121, 0xf, 0xf, false);
#pragma unroll
          for (int i = 0; i < 8; ++i) {
            const bool p = (pend >> i) & 1u;
            v[i].x = p ? t[i].x : v[i].x; v[i].y = p ? t[i].y : v[i].y; v[i].z = p ? t[i].z : v[i].z; v[i].w = p ? t[i].w : v[i].w;
          }
          pend &= badm;
          if (__all(pend == 0)) break;
          __builtin_amdgcn_s_sleep(H3_SLEEP);
        }
      }
      STAMP(7);
      float ls[8];
      float Mw = -3.0e38f;
#pragma unroll
      for (int i = 0; i < 8; ++i) { ls[i] = __uint_as_float(((v[i].y & TMASK) << 20) | ((v[i].w & TMASK) << 8)); Mw = fmaxf(Mw, ls[i]); }
      Mw = x32_max(x16_max(Mw));
      f32x4 o4 = {0.f, 0.f, 0.f, 0.f};
      float Lw = 0.f;
#pragma unroll
      for (int i = 0; i < 8; ++i) {
        const float ws_ = __builtin_amdgcn_exp2f(ls[i] - Mw);
        Lw += ws_;
        o4.x += ws_ * untag(v[i].x); o4.y += ws_ * untag(v[i].y); o4.z += ws_ * untag(v[i].z); o4.w += ws_ * untag(v[i].w);
      }
#pragma unroll
      for (int e2 = 0; e2 < 4; ++e2) o4[e2] = x32_add(x16_add(o4[e2]));
      Lw = x32_add(x16_add(Lw));
      if (g == 0) *(LAS f32x4*)(L + L_OL + w * 256 + c4 * 16) = o4;
      if (lane == 0) *(LAS v2u*)(L + L_SM + w * 8) = v2u{__float_as_uint(Mw), __float_as_uint(Lw)};
    }
    arrive(C_S5);
    await(C_S5, 4);
    {
      const int b = r >> 3;
      const f32x4 oa = LDF4(L_OL + (2 * b) * 256 + (16 * w + 4 * g) * 4), ob = LDF4(L_OL + (2 * b + 1) * 256 + (16 * w + 4 * g) * 4);
      const f32x4 ml = LDF4(L_SM + b * 16);
      const float Mb = fmaxf(ml.x, ml.z);
      const float ea = __builtin_amdgcn_exp2f(ml.x - Mb), eb = __builtin_amdgcn_exp2f(ml.z - Mb);
      const float inv = 1.f / (ml.y * ea + ml.w * eb);
      const f32x4 ov = oa * (ea * inv) + ob * (eb * inv);
      const unsigned h01 = pk_bf16(ov.x, ov.y), h23 = pk_bf16(ov.z, ov.w);
      const unsigned l01 = pk_bf16(ov.x - bf_lo(h01), ov.y - bf_hi(h01)), l23 = pk_bf16(ov.z - bf_lo(h23), ov.w - bf_hi(h23));
      f32x4 c0 = {0.f, 0.f, 0.f, 0.f}, c1 = {0.f, 0.f, 0.f, 0.f};
      c0 = mfma(buildA(h01, 0), wvv[0], c0); c1 = mfma(buildA(l01, 0), wvv[0], c1);
      c0 = mfma(buildA(h01, 1), wvv[1], c0); c1 = mfma(buildA(l01, 1), wvv[1], c1);
      c0 = mfma(buildA(h23, 0), wvv[2], c0); c1 = mfma(buildA(l23, 0), wvv[2], c1);
      c0 = mfma(buildA(h23, 1), wvv[3], c0); c1 = mfma(buildA(l23, 1), wvv[3], c1);
      *(LAS f32x4*)(L + L_CTX + w * 1024 + (g >> 1) * 512 + (8 * r + 4 * (g & 1)) * 4) = c0 + c1;
    }
    arrive(C_CTX);
    STAMP(8);
    if ((bid & 3) == 0) {
      const int oc = bid >> 2;
      v4u q[4] = {};
      const unsigned qbase = W_H4 + (unsigned)(64 * w + (lane >> 2)) * 4096u + oc * 64u + (lane & 3) * 16u;
      GATHER(4, q, qbase, 16u * 4096u, tag);
      STAMP(9);
      f32x4 t4;
#pragma unroll
      for (int e2 = 0; e2 < 4; ++e2) {
        float t = (untag(q[0][e2]) + untag(q[1][e2])) + (untag(q[2][e2]) + untag(q[3][e2]));
        t += dpp_ror<8>(t); t += dpp_ror<4>(t);
        t4[e2] = x32_add(x16_add(t));
      }
      if (lane < 4) *(LAS f32x4*)(L + L_SM + 256 + w * 64 + lane * 16) = t4;
      arrive(C_FIN);
      if (w == 0) {
        await(C_FIN, 4);
        if (lane < 4) {
          const f32x4 o = (LDF4(L_SM + 256 + lane * 16) + LDF4(L_SM + 320 + lane * 16)) + (LDF4(L_SM + 384 + lane * 16) + LDF4(L_SM + 448 + lane * 16));
          *(v2u*)(out + oc * 16 + lane * 4) = v2u{pk_bf16(o.x, o.y), pk_bf16(o.z, o.w)};
          if (bid == 0 && lane == 0) __builtin_amdgcn_raw_buffer_store_b32(epoch + 1u, rs, W_EPOCH, 0, 16);
        }
      }
    }
    STAMP(10);
    STAMP_FLUSH();
  } else {
    const int w = wave - 4;
    if (w == 0 && lane < 16) *(LAS unsigned*)(L + L_CNT + 4 * lane) = 0u;
    asm volatile("s_waitcnt lgkmcnt(0)" ::: "memory");
    FENCE();
    __builtin_amdgcn_s_barrier();
    FENCE();
    const u16* src = ckv + ((size_t)ab * 8192 + as * 128 + w * 32) * 512 + lane * 8;
#pragma unroll
    for (int i = 0; i < 32; ++i)
      __builtin_amdgcn_global_load_lds((const void*)(src + i * 512), (LAS void*)(L + L_KV + (w * 32 + i) * PITCH), 16, 0, 0);
    arrive_a(C_ISS);
    __builtin_amdgcn_s_waitcnt(0x0f70);
    arrive_a(C_KV);
    STAMP(1);
    await_a(C_S2, 4);
    v4u fv[8]; v4u fx;
    const bool ffn1 = (w < 2) && (bid < 224);
    const int cb = bid % 14, rb = bid / 14;
    if (ffn1) {
      const u16* fb = (w == 0 ? wg : wu) + (size_t)(32 * rb + 8 * g) * 1792 + 128 * cb + 8 * r;
#pragma unroll
      for (int i = 0; i < 8; ++i) fv[i] = ldnt(fb + (size_t)i * 1792);
      fx = *(const v4u*)(x + (r >> 3) * 512 + 32 * rb + 8 * g);
    }
    const v4u gx0 = *(const v4u*)(x + lane * 8), gx1 = *(const v4u*)(x + 512 + lane * 8), gwv = *(const v4u*)(gw + lane * 8);
    const unsigned epoch = __builtin_amdgcn_raw_buffer_load_b32(rs, vzero + W_EPOCH, 0, 16);
    v4u wov[34];
    const u16* wob = wo + ((size_t)h * 128 + 32 * g) * 512 + 128 * w + 8 * r;
#pragma unroll
    for (int i = 0; i < WO_A; ++i) wov[i] = ldnt(wob + ((i + 4 * j) & 31) * 512);
    {
      const int u = bid < 224 ? bid : 0;
      const u16* wdb = wd + (size_t)(8 * u + 2 * g) * 512 + 128 * w + 8 * r;
      wov[32] = ldnt(wdb);
      wov[33] = ldnt(wdb + 512);
    }
    FENCE();
    const unsigned tag = (epoch + 1u) & TMASK;
    if (ffn1) {
      f32x4 acc = {0.f, 0.f, 0.f, 0.f};
#pragma unroll
      for (int i = 0; i < 8; ++i) acc = mfma(buildA(fx[i >> 1], i & 1), fv[i], acc);
      const unsigned u = 16 * cb + r;
      __builtin_amdgcn_raw_buffer_store_b128(tag4(acc, tag), rs, W_HF + u * 2048 + ((w * 16 + rb) * 4 + g) * 16, 0, 16);
    }
    float gate0, gate1;
    {
      float s0 = 0.f, s1 = 0.f;
#pragma unroll
      for (int k = 0; k < 4; ++k) {
        s0 += bf_lo(gx0[k]) * bf_lo(gwv[k]) + bf_hi(gx0[k]) * bf_hi(gwv[k]);
        s1 += bf_lo(gx1[k]) * bf_lo(gwv[k]) + bf_hi(gx1[k]) * bf_hi(gwv[k]);
      }
      s0 = wave_sum(s0); s1 = wave_sum(s1);
      gate0 = 1.f / (1.f + __expf(-s0));
      gate1 = 1.f / (1.f + __expf(-s1));
    }
    STAMP(2);
    await_a(C_ST, 4);
    STAMP(5);
#pragma unroll
    for (int i = WO_A; i < 32; ++i) wov[i] = ldnt(wob + ((i + 4 * j) & 31) * 512);
    arrive_a(C_KVH);
    STAMP(6);
    unsigned hp = 0u, lp = 0u;
    if (bid < 224) {
      v4u ff[2] = {};
      const unsigned fbase = W_HF + bid * 2048 + lane * 16;
      GATHER(2, ff, fbase, 1024u, tag);
      const v4u f0 = ff[0], f1 = ff[1];
      f32x4 hv;
      const float og = 1.f - (((lane >> 1) & 1) ? gate1 : gate0);
#pragma unroll
      for (int e2 = 0; e2 < 4; ++e2) {
        float t = untag(f0[e2]); t += dpp_ror<8>(t); t += dpp_ror<4>(t); t = x32_add(x16_add(t));
        float u = untag(f1[e2]); u += dpp_ror<8>(u); u += dpp_ror<4>(u); u = x32_add(x16_add(u));
        const float a2 = 2.f * 0.7978845608028654f * (t + 0.044715f * t * t * t);
        hv[e2] = t / (1.f + __expf(-a2)) * u * og;
      }
      const int srcl = (r >> 3) * 2 + (g >> 1);
      const float a0 = __shfl(hv.x, srcl), a1 = __shfl(hv.y, srcl), a2_ = __shfl(hv.z, srcl), a3 = __shfl(hv.w, srcl);
      const float h0 = (g & 1) ? a2_ : a0, h1 = (g & 1) ? a3 : a1;
      hp = pk_bf16(h0, h1);
      lp = pk_bf16(h0 - bf_lo(hp), h1 - bf_hi(hp));
    }
    f32x4 acc = {0.f, 0.f, 0.f, 0.f};
    acc = mfma(buildA(hp, 0), wov[32], acc);
    acc = mfma(buildA(hp, 1), wov[33], acc);
    acc = mfma(buildA(lp, 0), wov[32], acc);
    acc = mfma(buildA(lp, 1), wov[33], acc);
    STAMP(7);
    await_a(C_CTX, 4);
    STAMP(8);
    {
      const int b = r >> 3;
      const float gb = b ? gate1 : gate0;
      const int off = L_CTX + b * 512 + (32 * g + 4 * jj) * 4;
      const f32x4 cs = ((LDF4(off) + LDF4(off + 1024)) + (LDF4(off + 2048) + LDF4(off + 3072))) * gb;
      *(LAS v2u*)(L + L_PK + w * 512 + b * 256 + (16 * g + 2 * jj) * 4) = v2u{pk_bf16(cs.x, cs.y), pk_bf16(cs.z, cs.w)};
      FENCE();
      v2u pk[8];
#pragma unroll
      for (int k = 0; k < 8; ++k) pk[k] = *(const LAS v2u*)(L + L_PK + w * 512 + b * 256 + (16 * g + ((2 * k + 2 * j) & 15)) * 4);
#pragma unroll
      for (int i = 0; i < 32; ++i) acc = mfma(buildA(pk[i >> 2][(i >> 1) & 1], i & 1), wov[i], acc);
    }
    const unsigned cons = (g >> 1) * 128 + 32 * w + 2 * r + (g & 1);
    __builtin_amdgcn_raw_buffer_store_b128(tag4(acc, tag), rs, W_H4 + bid * 4096 + cons * 16, 0, 16);
    STAMP(9);
    STAMP_FLUSH();
  }
}
}

extern "C" void kernel_launch(void* const* d_in, const int* in_sizes, int n_in, void* d_out, int out_size, void* d_ws, size_t ws_size,
                              hipStream_t stream) {
  (void)in_sizes; (void)n_in; (void)out_size; (void)ws_size;
  (void)hipFuncSetAttribute((const void*)mk, hipFuncAttributeMaxDynamicSharedMemorySize, L_ALLOC);
  mk<<<256, 512, L_ALLOC, stream>>>((const u16*)d_in[0], (const u16*)d_in[1], (const u16*)d_in[2], (const u16*)d_in[3], (const u16*)d_in[4],
                                  (const u16*)d_in[5], (const u16*)d_in[7], (const u16*)d_in[8], (const u16*)d_in[9], (const u16*)d_in[10],
                                  (u16*)d_out, (unsigned char*)d_ws);
}
```

```cpp
#include <hip/hip_runtime.h>
#include <hip/hip_bf16.h>
#include <stdint.h>

typedef unsigned short u16;
typedef unsigned v4u __attribute__((ext_vector_type(4)));
typedef unsigned v2u __attribute__((ext_vector_type(2)));
typedef float f32x4 __attribute__((ext_vector_type(4)));
typedef short s16x4 __attribute__((ext_vector_type(4)));
typedef __bf16 bf16x8 __attribute__((ext_vector_type(8)));
#define LAS __attribute__((address_space(3)))
#define GAS __attribute__((address_space(1)))

namespace {
constexpr int PITCH = 1056;
constexpr int L_KV = 0;
constexpr int L_Q = 128 * PITCH;
constexpr int L_P = L_Q + 16 * PITCH;
constexpr int L_SM = L_P + 4608;
constexpr int L_OL = L_Q;
constexpr int L_CTX = L_OL + 1024;
constexpr int L_PK = L_CTX + 4096;
constexpr int L_CNT = L_SM + 512;
constexpr int L_END = L_CNT + 64;
static_assert(L_END <= 160 * 1024, "LDS");
enum { C_S1 = 0, C_S2, C_KV, C_S3, C_S4, C_S5, C_CTX, C_ST, C_FIN, C_KVH, C_PUB, C_ISS };
constexpr unsigned OOB = 0xfffffff0u;

constexpr unsigned W_EPOCH = 0;
constexpr unsigned W_H2 = 4096;
constexpr unsigned W_HF = W_H2 + 2 * 2 * 32 * 512 * 4;
constexpr unsigned W_H4 = W_HF + 224 * 2048;
constexpr unsigned W_H3 = W_H4 + 256 * 256 * 16;
constexpr unsigned W_DBG = W_H3 + 32 * 8 * 2 * 64 * 64 * 4;
constexpr unsigned W_END = W_DBG + 256 * 2 * 16 * 8;
#define H3_SLEEP 8
#define H2_DELAY 20
#define H3_GATE 1
#define ISS_AT 20
#define TLBW 1
#define DMA0 8
#define WO_B 32
#define PRIO 3
#define HOLD2 0
#define CODE_KB 22
#define KVH_N 20
#define WO_A 4
constexpr int SPIN_CAP = 1 << 15;
constexpr int LSPIN_CAP = 1 << 22;

__device__ __forceinline__ f32x4 mfma(v4u a, v4u b, f32x4 c) {
  return __builtin_amdgcn_mfma_f32_16x16x32_bf16(__builtin_bit_cast(bf16x8, a), __builtin_bit_cast(bf16x8, b), c, 0, 0, 0);
}
__device__ __forceinline__ float bf_lo(unsigned w) { return __uint_as_float(w << 16); }
__device__ __forceinline__ float bf_hi(unsigned w) { return __uint_as_float(w & 0xffff0000u); }
__device__ __forceinline__ unsigned pk_bf16(float a, float b) {
  typedef __bf16 bf16x2 __attribute__((ext_vector_type(2)));
  typedef float f32x2 __attribute__((ext_vector_type(2)));
  f32x2 v = {a, b};
  bf16x2 r = __builtin_convertvector(v, bf16x2);
  return __builtin_bit_cast(unsigned, r);
}
constexpr unsigned TMASK = 0xfffu;
__device__ __forceinline__ unsigned tagw(float v, unsigned tag) { return ((__float_as_uint(v) + 0x800u) & ~TMASK) | tag; }
__device__ __forceinline__ float untag(unsigned w) { return __uint_as_float(w & ~TMASK); }
__device__ __forceinline__ v4u tag4(f32x4 v, unsigned tag) { return v4u{tagw(v.x, tag), tagw(v.y, tag), tagw(v.z, tag), tagw(v.w, tag)}; }
__device__ __forceinline__ unsigned bad4(v4u w, unsigned tag) { return ((w.x ^ tag) | (w.y ^ tag) | (w.z ^ tag) | (w.w ^ tag)) & TMASK; }
template <int N> __device__ __forceinline__ float dpp_ror(float v) {
  return __int_as_float(__builtin_amdgcn_update_dpp(0, __float_as_int(v), 0x120 + N, 0xf, 0xf, false));
}
__device__ __forceinline__ float x16_add(float v) { auto p = __builtin_amdgcn_permlane16_swap(__float_as_uint(v), __float_as_uint(v), false, false); return __uint_as_float(p[0]) + __uint_as_float(p[1]); }
__device__ __forceinline__ float x32_add(float v) { auto p = __builtin_amdgcn_permlane32_swap(__float_as_uint(v), __float_as_uint(v), false, false); return __uint_as_float(p[0]) + __uint_as_float(p[1]); }
__device__ __forceinline__ float x16_max(float v) { auto p = __builtin_amdgcn_permlane16_swap(__float_as_uint(v), __float_as_uint(v), false, false); return fmaxf(__uint_as_float(p[0]), __uint_as_float(p[1])); }
__device__ __forceinline__ float x32_max(float v) { auto p = __builtin_amdgcn_permlane32_swap(__float_as_uint(v), __float_as_uint(v), false, false); return fmaxf(__uint_as_float(p[0]), __uint_as_float(p[1])); }
__device__ __forceinline__ float wave_sum(float v) { v += dpp_ror<8>(v); v += dpp_ror<4>(v); v += dpp_ror<2>(v); v += dpp_ror<1>(v); v = x16_add(v); return x32_add(v); }
__device__ __forceinline__ float wave_max(float v) { v = fmaxf(v, dpp_ror<8>(v)); v = fmaxf(v, dpp_ror<4>(v)); v = fmaxf(v, dpp_ror<2>(v)); v = fmaxf(v, dpp_ror<1>(v)); v = x16_max(v); return x32_max(v); }
__device__ __forceinline__ v4u ldnt(const u16* p) { return *(const v4u*)p; }

#define LD128(off) (*(const LAS v4u*)(L + (off)))
#define LDF4(off) (*(const LAS f32x4*)(L + (off)))
#define FENCE() do { __builtin_amdgcn_sched_barrier(0); asm volatile("" ::: "memory"); } while (0)

#define STAMP(i)
#define STAMP_FLUSH()
#define L_ALLOC L_END

__global__ __launch_bounds__(512, 2) void mk(const u16* __restrict__ x, const u16* __restrict__ ckv, const u16* __restrict__ wq,
                                             const u16* __restrict__ wuk, const u16* __restrict__ wuv, const u16* __restrict__ wo,
                                             const u16* __restrict__ wg, const u16* __restrict__ wu, const u16* __restrict__ wd,
                                             const u16* __restrict__ gw, u16* __restrict__ out, unsigned char* ws) {
  unsigned long long pc0;
  asm volatile("s_getpc_b64 %0" : "=s"(pc0));
  extern __shared__ __attribute__((aligned(16))) unsigned char lds[];
  LAS unsigned char* L = (LAS unsigned char*)lds;
  const int tid = threadIdx.x, lane = tid & 63, wave = __builtin_amdgcn_readfirstlane(tid >> 6);
  const int bid = blockIdx.x, xcd = bid & 7, slot = bid >> 3;
  const int h = xcd * 4 + (slot >> 3), j = slot & 7;
  const int ab = xcd >> 2, as = (xcd & 3) * 16 + (slot >> 1), hg = slot & 1;
  const int g = lane >> 4, r = lane & 15, jj = r & 7;
  const auto rs = __builtin_amdgcn_make_buffer_rsrc(ws, 0, W_END, 0x00020000);
  STAMP(0);
  int vzero = 0;
  asm volatile("" : "+v"(vzero));
  unsigned selE[4], selO[4];
#pragma unroll
  for (int k = 0; k < 4; ++k) {
    selE[k] = (k == (jj >> 1)) ? ((jj & 1) ? 0x01000c0cu : 0x0c0c0100u) : 0x0c0c0c0cu;
    selO[k] = (k == (jj >> 1)) ? ((jj & 1) ? 0x03020c0cu : 0x0c0c0302u) : 0x0c0c0c0cu;
  }
  auto buildA = [&](unsigned pw, int odd) -> v4u {
    v4u a;
    if (odd) { a.x = __builtin_amdgcn_perm(pw, pw, selO[0]); a.y = __builtin_amdgcn_perm(pw, pw, selO[1]); a.z = __builtin_amdgcn_perm(pw, pw, selO[2]); a.w = __builtin_amdgcn_perm(pw, pw, selO[3]); }
    else     { a.x = __builtin_amdgcn_perm(pw, pw, selE[0]); a.y = __builtin_amdgcn_perm(pw, pw, selE[1]); a.z = __builtin_amdgcn_perm(pw, pw, selE[2]); a.w = __builtin_amdgcn_perm(pw, pw, selE[3]); }
    return a;
  };
  auto arrive = [&](int c) {
    FENCE();
    if (lane == 0) __hip_atomic_fetch_add((LAS unsigned*)(L + L_CNT + 4 * c), 1u, __ATOMIC_RELAXED, __HIP_MEMORY_SCOPE_WORKGROUP);
    FENCE();
  };
  auto await = [&](int c, unsigned n) {
    FENCE();
    for (int s = 0; s < LSPIN_CAP; ++s) {
      if (*(volatile LAS unsigned*)(L + L_CNT + 4 * c) >= n) break;
    }
    FENCE();
  };

  auto arrive_a = [&](int c) {
    FENCE();
    if (lane == 0) asm volatile("ds_add_u32 %0, %1" ::"v"((unsigned)(L_CNT + 4 * c)), "v"(1u) : "memory");
    FENCE();
  };
  auto await_a = [&](int c, unsigned n) {
    FENCE();
    for (int s = 0; s < LSPIN_CAP; ++s) {
      unsigned v;
      asm volatile("ds_read_b32 %0, %1\n\ts_waitcnt lgkmcnt(0)" : "=v"(v) : "v"((unsigned)(L_CNT + 4 * c)) : "memory");
      if ((unsigned)__builtin_amdgcn_readfirstlane((int)v) >= n) break;
    }
    FENCE();
  };
#define GATHER(N, v, base, stride, tagv) GATHERX(N, v, base, stride, tagv, , 0u)
#define GATHERX(N, v, base, stride, tagv, EXTRA_LOAD, EXTRA_BAD)                                                \
  {                                                                                                             \
    unsigned pend_ = (1u << (N)) - 1u;                                                                          \
    for (int spin_ = 0; spin_ < SPIN_CAP; ++spin_) {                                                            \
      asm volatile("" ::: "memory");                                                                            \
      v4u t_[N];                                                                                                \
      EXTRA_LOAD;                                                                                               \
      _Pragma("unroll") for (int i_ = 0; i_ < (N); ++i_)                                                        \
        t_[i_] = __builtin_amdgcn_raw_buffer_load_b128(rs, ((pend_ >> i_) & 1u) ? (base) + i_ * (stride) : OOB, 0, 16); \
      _Pragma("unroll") for (int i_ = 0; i_ < (N); ++i_) {                                                      \
        const bool p_ = (pend_ >> i_) & 1u;                                                                     \
        v[i_].x = p_ ? t_[i_].x : v[i_].x; v[i_].y = p_ ? t_[i_].y : v[i_].y;                                   \
        v[i_].z = p_ ? t_[i_].z : v[i_].z; v[i_].w = p_ ? t_[i_].w : v[i_].w;                                   \
        if (bad4(v[i_], tagv) == 0) pend_ &= ~(1u << i_);                                                       \
      }                                                                                                         \
      if (__all((pend_ | (EXTRA_BAD)) == 0)) break;                                                             \
      __builtin_amdgcn_s_sleep(1);                                                                              \
    }                                                                                                           \
  }

  if (wave < 4) {
    const int w = wave;
    __builtin_amdgcn_s_setprio(PRIO);
    unsigned tag, epoch;
    {
      const int dq = j & 1, cq = j >> 1;
      const u16* wkb = wuk + ((size_t)(128 * cq + 32 * w + r) * 32 + h) * 128 + 64 * dq + 8 * g;
      v4u wk[4];
#pragma unroll
      for (int f = 0; f < 4; ++f) wk[f] = ldnt(wkb + (size_t)(f >> 1) * (16 * 4096) + 32 * (f & 1));
      const u16* xp = x + (r >> 3) * 512 + 128 * w + 32 * g;
      v2u xv[8];
#pragma unroll
      for (int k = 0; k < 8; ++k) xv[k] = *(const v2u*)(xp + 16 * (k >> 2) + ((4 * k + 4 * cq) & 15));
      const u16* wqb = wq + (size_t)(128 * w + 32 * g + 16 * (r >> 3)) * 4096 + h * 128 + 64 * dq + 8 * (r & 7);
      v4u wv[16];
#pragma unroll
      for (int i = 0; i < 16; ++i) wv[i] = ldnt(wqb + (size_t)((i + 4 * cq) & 15) * 4096);
      epoch = __builtin_amdgcn_raw_buffer_load_b32(rs, vzero + W_EPOCH, 0, 16);
      FENCE();
      __builtin_amdgcn_s_barrier();
      FENCE();
      f32x4 acc = {0.f, 0.f, 0.f, 0.f}, acc2 = {0.f, 0.f, 0.f, 0.f};
#pragma unroll
      for (int i = 0; i < 16; ++i) {
        acc = mfma(buildA(xv[i >> 2][(i >> 1) & 1], i & 1), wv[i], acc);
        acc2 = mfma(buildA(xv[4 + (i >> 2)][(i >> 1) & 1], i & 1), wv[i], acc2);
      }
#pragma unroll
      for (int e = 0; e < 4; ++e) { float v = (r < 8) ? acc[e] : acc2[e]; acc[e] = v + dpp_ror<8>(v); }
      STAMP(1);
      if (r < 8) {
        const unsigned h01 = pk_bf16(acc.x, acc.y), h23 = pk_bf16(acc.z, acc.w);
        const unsigned l01 = pk_bf16(acc.x - bf_lo(h01), acc.y - bf_hi(h01)), l23 = pk_bf16(acc.z - bf_lo(h23), acc.w - bf_hi(h23));
        const int b = g >> 1, d0 = 8 * r + 4 * (g & 1);
        *(LAS v2u*)(L + L_P + (b + 4 * w) * 160 + d0 * 2) = v2u{h01, h23};
        *(LAS v2u*)(L + L_P + (b + 2 + 4 * w) * 160 + d0 * 2) = v2u{l01, l23};
      }
      arrive(C_S1);
      await(C_S1, 4);
      const v4u qb0 = LD128(L_P + r * 160 + g * 16), qb1 = LD128(L_P + r * 160 + 64 + g * 16);
      f32x4 qa = {0.f, 0.f, 0.f, 0.f}, qc = {0.f, 0.f, 0.f, 0.f};
      qa = mfma(wk[0], qb0, qa); qc = mfma(wk[2], qb0, qc);
      qa = mfma(wk[1], qb1, qa); qc = mfma(wk[3], qb1, qc);
#pragma unroll
      for (int e = 0; e < 4; ++e) {
        float v = qa[e]; v += dpp_ror<8>(v); v += dpp_ror<4>(v); v += dpp_ror<2>(v);
        float u = qc[e]; u += dpp_ror<8>(u); u += dpp_ror<4>(u); u += dpp_ror<2>(u);
        qa[e] = ((r & 2) ? u : v) * (0.08838834764831845f * 1.4426950408889634f);
      }
      tag = (epoch + 1u) & TMASK;
      if (r < 4)
        __builtin_amdgcn_raw_buffer_store_b128(tag4(qa, tag), rs,
            W_H2 + (((((r & 1) * 2 + (h >> 4)) * 2 + dq) * 16 + (h & 15)) * 512 + 128 * cq + 32 * w + 16 * (r >> 1) + 4 * g) * 4, 0, 16);
    }
    STAMP(2);
    await(C_ISS, 4);
    STAMP(3);
    v4u kf[32];
    {
      v4u v[16];
      const unsigned base = W_H2 + (unsigned)(ab * 2 + hg) * 65536u + (unsigned)tid * 16u;
#pragma unroll
      for (int i = 0; i < 16; ++i) v[i] = __builtin_amdgcn_raw_buffer_load_b128(rs, base + i * 4096u, 0, 16);
      await(C_KV, 4);
      STAMP(12);
#pragma unroll
      for (int ks = 0; ks < 16; ++ks) {
        kf[2 * ks] = LD128(L_KV + (32 * w + r) * PITCH + ks * 64 + g * 16);
        kf[2 * ks + 1] = LD128(L_KV + (32 * w + 16 + r) * PITCH + ks * 64 + g * 16);
      }
      FENCE();
      unsigned bad0 = 0;
#pragma unroll
      for (int i = 0; i < 16; ++i) bad0 |= bad4(v[i], tag);
      if (!__all(bad0 == 0)) {
        for (int spin = 0; spin < SPIN_CAP; ++spin) {
          asm volatile("" ::: "memory");
#pragma unroll
          for (int i = 0; i < 16; ++i) v[i] = __builtin_amdgcn_raw_buffer_load_b128(rs, base + i * 4096u, 0, 16);
          unsigned bad = 0;
#pragma unroll
          for (int i = 0; i < 16; ++i) bad |= bad4(v[i], tag);
          if (__all(bad == 0)) break;
        }
        FENCE();
#pragma unroll
        for (int ks = 0; ks < 16; ++ks) {
          kf[2 * ks] = LD128(L_KV + (32 * w + r) * PITCH + ks * 64 + g * 16);
          kf[2 * ks + 1] = LD128(L_KV + (32 * w + 16 + r) * PITCH + ks * 64 + g * 16);
        }
      }
#pragma unroll
      for (int i = 0; i < 8; ++i) {
        const int head = 2 * i + (tid >> 7), c = 4 * (tid & 127);
        *(LAS v2u*)(L + L_Q + head * PITCH + c * 2) =
            v2u{pk_bf16(untag(v[i].x) + untag(v[i + 8].x), untag(v[i].y) + untag(v[i + 8].y)), pk_bf16(untag(v[i].z) + untag(v[i + 8].z), untag(v[i].w) + untag(v[i + 8].w))};
      }
    }
    STAMP(11);
    arrive(C_S2);
    v4u wvv[4];
    {
      const u16* wvb = wuv + ((size_t)(64 * j + 16 * w + 4 * g) * 32 + h) * 128 + 8 * r;
#pragma unroll
      for (int i = 0; i < 4; ++i) wvv[i] = ldnt(wvb + (size_t)i * 4096);
    }
    await(C_S2, 4);
    STAMP(4);
    {
      f32x4 s0 = {0.f, 0.f, 0.f, 0.f}, s1 = {0.f, 0.f, 0.f, 0.f};
      const int tq = r >> 2, tp = r & 3;
      v4u vf[32];
      {
        v4u qf[16];
#pragma unroll
        for (int ks = 0; ks < 16; ++ks) qf[ks] = LD128(L_Q + r * PITCH + ks * 64 + g * 16);
        FENCE();
#pragma unroll
        for (int ks = 0; ks < 16; ++ks) {
          s0 = mfma(kf[2 * ks], qf[ks], s0);
          s1 = mfma(kf[2 * ks + 1], qf[ks], s1);
#pragma unroll
          for (int e = 0; e < 2; ++e) {
            const int ct = ks >> 1, i = 2 * (ks & 1) + e;
            const int a0 = L_KV + (32 * i + 4 * g + tq) * PITCH + (128 * w + 16 * ct + 4 * tp) * 2;
            const v2u t0 = __builtin_bit_cast(v2u, __builtin_amdgcn_ds_read_tr16_b64_v4i16((LAS s16x4*)(L + a0)));
            const v2u t1 = __builtin_bit_cast(v2u, __builtin_amdgcn_ds_read_tr16_b64_v4i16((LAS s16x4*)(L + a0 + 16 * PITCH)));
            vf[4 * ct + i] = v4u{t0.x, t0.y, t1.x, t1.y};
          }
          __builtin_amdgcn_sched_barrier(0);
        }
      }
      FENCE();
      float mx = fmaxf(fmaxf(fmaxf(s0.x, s0.y), fmaxf(s0.z, s0.w)), fmaxf(fmaxf(s1.x, s1.y), fmaxf(s1.z, s1.w)));
      mx = x32_max(x16_max(mx));
      if (g == 0) *(LAS float*)(L + L_SM + (r * 4 + w) * 4) = mx;
      arrive(C_S3);
      await(C_S3, 4);
      const f32x4 m0 = LDF4(L_SM + r * 16);
      float M = fmaxf(fmaxf(m0.x, m0.y), fmaxf(m0.z, m0.w));
      f32x4 p0, p1;
#pragma unroll
      for (int e = 0; e < 4; ++e) { p0[e] = __builtin_amdgcn_exp2f(s0[e] - M); p1[e] = __builtin_amdgcn_exp2f(s1[e] - M); }
      *(LAS v4u*)(L + L_P + w * 1024 + lane * 16) = v4u{pk_bf16(p0.x, p0.y), pk_bf16(p0.z, p0.w), pk_bf16(p1.x, p1.y), pk_bf16(p1.z, p1.w)};
      float ls = ((p0.x + p0.y) + (p0.z + p0.w)) + ((p1.x + p1.y) + (p1.z + p1.w));
      ls = x32_add(x16_add(ls));
      if (g == 0) *(LAS float*)(L + L_SM + 256 + (r * 4 + w) * 4) = ls;
      arrive(C_S4);
      await(C_S4, 4);
      STAMP(5);
      v4u pf[4];
#pragma unroll
      for (int i = 0; i < 4; ++i) pf[i] = LD128(L_P + i * 1024 + lane * 16);
      unsigned lsb;
      float osc;
      {
        const f32x4 l0 = LDF4(L_SM + 256 + r * 16);
        const float lse = M + __builtin_amdgcn_logf((l0.x + l0.y) + (l0.z + l0.w));
        lsb = (__float_as_uint(lse) + 0x80u) & 0xffffff00u;
        osc = __builtin_amdgcn_exp2f(M - __uint_as_float(lsb));
      }
      const unsigned f1 = lsb >> 20, f3 = (lsb >> 8) & TMASK;
      const int stg = L_KV + w * (32 * 272);
#pragma unroll
      for (int ct = 0; ct < 8; ++ct) {
        f32x4 o = {0.f, 0.f, 0.f, 0.f};
#pragma unroll
        for (int i = 0; i < 4; ++i) o = mfma(vf[4 * ct + i], pf[i], o);
        o *= osc;
        *(LAS v4u*)(L + stg + ((ct >> 2) * 16 + r) * 272 + ((ct & 3) * 4 + g) * 16) = v4u{tagw(o.x, tag), tagw(o.y, f1), tagw(o.z, tag), tagw(o.w, f3)};
      }
      FENCE();
#pragma unroll
      for (int k = 0; k < 8; ++k) {
        const v4u d = LD128(stg + (4 * k + g) * 272 + r * 16);
        __builtin_amdgcn_raw_buffer_store_b128(d, rs, W_H3 + (((ab * 64 + as) * 2 + hg) * 32 + 8 * w + k) * 1024 + lane * 16, 0, 16);
      }
    }
    arrive(C_ST);
    STAMP(6);
    await(C_KVH, 4);
    {
      const int b = w >> 1;
      const int c4 = tid & 15, sg = (tid >> 4) & 7;
      v4u v[8] = {};
      const unsigned obase = W_H3 + (unsigned)(((b * 64 + sg) * 2 + (h >> 4)) * 32 + 4 * j) * 1024u + (h & 15) * 256u + c4 * 16u;
      {
        unsigned pend = 0xffu;
        for (int spin = 0; spin < SPIN_CAP; ++spin) {
          asm volatile("" ::: "memory");
          v4u t[8];
#pragma unroll
          for (int i = 0; i < 8; ++i) t[i] = __builtin_amdgcn_raw_buffer_load_b128(rs, ((pend >> i) & 1u) ? obase + i * (8u * 65536u) : OOB, 0, 16);
          unsigned badm = 0;
#pragma unroll
          for (int i = 0; i < 8; ++i) badm |= ((((t[i].x ^ tag) | (t[i].z ^ tag)) & TMASK) ? 1u : 0u) << i;
          badm |= (unsigned)__builtin_amdgcn_update_dpp(0, (int)badm, 0x128, 0xf, 0xf, false);
          badm |= (unsigned)__builtin_amdgcn_update_dpp(0, (int)badm, 0x124, 0xf, 0xf, false);
          badm |= (unsigned)__builtin_amdgcn_update_dpp(0, (int)badm, 0x122, 0xf, 0xf, false);
          badm |= (unsigned)__builtin_amdgcn_update_dpp(0, (int)badm, 0x121, 0xf, 0xf, false);
#pragma unroll
          for (int i = 0; i < 8; ++i) {
            const bool p = (pend >> i) & 1u;
            v[i].x = p ? t[i].x : v[i].x; v[i].y = p ? t[i].y : v[i].y; v[i].z = p ? t[i].z : v[i].z; v[i].w = p ? t[i].w : v[i].w;
          }
          pend &= badm;
          if (__all(pend == 0)) break;
          __builtin_amdgcn_s_sleep(H3_SLEEP);
        }
      }
      STAMP(7);
      float ls[8];
      float Mw = -3.0e38f;
#pragma unroll
      for (int i = 0; i < 8; ++i) { ls[i] = __uint_as_float(((v[i].y & TMASK) << 20) | ((v[i].w & TMASK) << 8)); Mw = fmaxf(Mw, ls[i]); }
      Mw = x32_max(x16_max(Mw));
      f32x4 o4 = {0.f, 0.f, 0.f, 0.f};
      float Lw = 0.f;
#pragma unroll
      for (int i = 0; i < 8; ++i) {
        const float ws_ = __builtin_amdgcn_exp2f(ls[i] - Mw);
        Lw += ws_;
        o4.x += ws_ * untag(v[i].x); o4.y += ws_ * untag(v[i].y); o4.z += ws_ * untag(v[i].z); o4.w += ws_ * untag(v[i].w);
      }
#pragma unroll
      for (int e2 = 0; e2 < 4; ++e2) o4[e2] = x32_add(x16_add(o4[e2]));
      Lw = x32_add(x16_add(Lw));
      if (g == 0) *(LAS f32x4*)(L + L_OL + w * 256 + c4 * 16) = o4;
      if (lane == 0) *(LAS v2u*)(L + L_SM + w * 8) = v2u{__float_as_uint(Mw), __float_as_uint(Lw)};
    }
    arrive(C_S5);
    await(C_S5, 4);
    {
      const int b = r >> 3;
      const f32x4 oa = LDF4(L_OL + (2 * b) * 256 + (16 * w + 4 * g) * 4), ob = LDF4(L_OL + (2 * b + 1) * 256 + (16 * w + 4 * g) * 4);
      const f32x4 ml = LDF4(L_SM + b * 16);
      const float Mb = fmaxf(ml.x, ml.z);
      const float ea = __builtin_amdgcn_exp2f(ml.x - Mb), eb = __builtin_amdgcn_exp2f(ml.z - Mb);
      const float inv = 1.f / (ml.y * ea + ml.w * eb);
      const f32x4 ov = oa * (ea * inv) + ob * (eb * inv);
      const unsigned h01 = pk_bf16(ov.x, ov.y), h23 = pk_bf16(ov.z, ov.w);
      const unsigned l01 = pk_bf16(ov.x - bf_lo(h01), ov.y - bf_hi(h01)), l23 = pk_bf16(ov.z - bf_lo(h23), ov.w - bf_hi(h23));
      f32x4 c0 = {0.f, 0.f, 0.f, 0.f}, c1 = {0.f, 0.f, 0.f, 0.f};
      c0 = mfma(buildA(h01, 0), wvv[0], c0); c1 = mfma(buildA(l01, 0), wvv[0], c1);
      c0 = mfma(buildA(h01, 1), wvv[1], c0); c1 = mfma(buildA(l01, 1), wvv[1], c1);
      c0 = mfma(buildA(h23, 0), wvv[2], c0); c1 = mfma(buildA(l23, 0), wvv[2], c1);
      c0 = mfma(buildA(h23, 1), wvv[3], c0); c1 = mfma(buildA(l23, 1), wvv[3], c1);
      *(LAS f32x4*)(L + L_CTX + w * 1024 + (g >> 1) * 512 + (8 * r + 4 * (g & 1)) * 4) = c0 + c1;
    }
    arrive(C_CTX);
    STAMP(8);
    if ((bid & 3) == 0) {
      const int oc = bid >> 2;
      v4u q[4] = {};
      const unsigned qbase = W_H4 + (unsigned)(64 * w + (lane >> 2)) * 4096u + oc * 64u + (lane & 3) * 16u;
      GATHER(4, q, qbase, 16u * 4096u, tag);
      STAMP(9);
      f32x4 t4;
#pragma unroll
      for (int e2 = 0; e2 < 4; ++e2) {
        float t = (untag(q[0][e2]) + untag(q[1][e2])) + (untag(q[2][e2]) + untag(q[3][e2]));
        t += dpp_ror<8>(t); t += dpp_ror<4>(t);
        t4[e2] = x32_add(x16_add(t));
      }
      if (lane < 4) *(LAS f32x4*)(L + L_SM + 256 + w * 64 + lane * 16) = t4;
      arrive(C_FIN);
      if (w == 0) {
        await(C_FIN, 4);
        if (lane < 4) {
          const f32x4 o = (LDF4(L_SM + 256 + lane * 16) + LDF4(L_SM + 320 + lane * 16)) + (LDF4(L_SM + 384 + lane * 16) + LDF4(L_SM + 448 + lane * 16));
          *(v2u*)(out + oc * 16 + lane * 4) = v2u{pk_bf16(o.x, o.y), pk_bf16(o.z, o.w)};
          if (bid == 0 && lane == 0) __builtin_amdgcn_raw_buffer_store_b32(epoch + 1u, rs, W_EPOCH, 0, 16);
        }
      }
    }
    STAMP(10);
    STAMP_FLUSH();
  } else {
    const int w = wave - 4;
    if (w == 0 && lane < 16) *(LAS unsigned*)(L + L_CNT + 4 * lane) = 0u;
    if (w == 3 && slot < CODE_KB) {
      const v4u cv = *(const v4u*)((const unsigned char*)(pc0 & ~255ull) + slot * 1024 + lane * 16);
      asm volatile("" ::"v"(cv));
    }
    asm volatile("s_waitcnt lgkmcnt(0)" ::: "memory");
    FENCE();
    __builtin_amdgcn_s_barrier();
    FENCE();
    const u16* src = ckv + ((size_t)ab * 8192 + as * 128 + w * 32) * 512 + lane * 8;
#pragma unroll
    for (int i = 0; i < 32; ++i)
      __builtin_amdgcn_global_load_lds((const void*)(src + i * 512), (LAS void*)(L + L_KV + (w * 32 + i) * PITCH), 16, 0, 0);
    arrive_a(C_ISS);
    __builtin_amdgcn_s_waitcnt(0x0f70);
    arrive_a(C_KV);
    STAMP(1);
    await_a(C_S2, 4);
    v4u fv[8]; v4u fx;
    const bool ffn1 = (w < 2) && (bid < 224);
    const int cb = bid % 14, rb = bid / 14;
    if (ffn1) {
      const u16* fb = (w == 0 ? wg : wu) + (size_t)(32 * rb + 8 * g) * 1792 + 128 * cb + 8 * r;
#pragma unroll
      for (int i = 0; i < 8; ++i) fv[i] = ldnt(fb + (size_t)i * 1792);
      fx = *(const v4u*)(x + (r >> 3) * 512 + 32 * rb + 8 * g);
    }
    const v4u gx0 = *(const v4u*)(x + lane * 8), gx1 = *(const v4u*)(x + 512 + lane * 8), gwv = *(const v4u*)(gw + lane * 8);
    const unsigned epoch = __builtin_amdgcn_raw_buffer_load_b32(rs, vzero + W_EPOCH, 0, 16);
    v4u wov[34];
    const u16* wob = wo + ((size_t)h * 128 + 32 * g) * 512 + 128 * w + 8 * r;
#pragma unroll
    for (int i = 0; i < WO_A; ++i) wov[i] = ldnt(wob + ((i + 4 * j) & 31) * 512);
    {
      const int u = bid < 224 ? bid : 0;
      const u16* wdb = wd + (size_t)(8 * u + 2 * g) * 512 + 128 * w + 8 * r;
      wov[32] = ldnt(wdb);
      wov[33] = ldnt(wdb + 512);
    }
    FENCE();
    const unsigned tag = (epoch + 1u) & TMASK;
    if (ffn1) {
      f32x4 acc = {0.f, 0.f, 0.f, 0.f};
#pragma unroll
      for (int i = 0; i < 8; ++i) acc = mfma(buildA(fx[i >> 1], i & 1), fv[i], acc);
      const unsigned u = 16 * cb + r;
      __builtin_amdgcn_raw_buffer_store_b128(tag4(acc, tag), rs, W_HF + u * 2048 + ((w * 16 + rb) * 4 + g) * 16, 0, 16);
    }
    float gate0, gate1;
    {
      float s0 = 0.f, s1 = 0.f;
#pragma unroll
      for (int k = 0; k < 4; ++k) {
        s0 += bf_lo(gx0[k]) * bf_lo(gwv[k]) + bf_hi(gx0[k]) * bf_hi(gwv[k]);
        s1 += bf_lo(gx1[k]) * bf_lo(gwv[k]) + bf_hi(gx1[k]) * bf_hi(gwv[k]);
      }
      s0 = wave_sum(s0); s1 = wave_sum(s1);
      gate0 = 1.f / (1.f + __expf(-s0));
      gate1 = 1.f / (1.f + __expf(-s1));
    }
    STAMP(2);
    await_a(C_ST, 4);
    STAMP(5);
#pragma unroll
    for (int i = WO_A; i < 32; ++i) wov[i] = ldnt(wob + ((i + 4 * j) & 31) * 512);
    arrive_a(C_KVH);
    STAMP(6);
    unsigned hp = 0u, lp = 0u;
    if (bid < 224) {
      v4u ff[2] = {};
      const unsigned fbase = W_HF + bid * 2048 + lane * 16;
      GATHER(2, ff, fbase, 1024u, tag);
      const v4u f0 = ff[0], f1 = ff[1];
      f32x4 hv;
      const float og = 1.f - (((lane >> 1) & 1) ? gate1 : gate0);
#pragma unroll
      for (int e2 = 0; e2 < 4; ++e2) {
        float t = untag(f0[e2]); t += dpp_ror<8>(t); t += dpp_ror<4>(t); t = x32_add(x16_add(t));
        float u = untag(f1[e2]); u += dpp_ror<8>(u); u += dpp_ror<4>(u); u = x32_add(x16_add(u));
        const float a2 = 2.f * 0.7978845608028654f * (t + 0.044715f * t * t * t);
        hv[e2] = t / (1.f + __expf(-a2)) * u * og;
      }
      const int srcl = (r >> 3) * 2 + (g >> 1);
      const float a0 = __shfl(hv.x, srcl), a1 = __shfl(hv.y, srcl), a2_ = __shfl(hv.z, srcl), a3 = __shfl(hv.w, srcl);
      const float h0 = (g & 1) ? a2_ : a0, h1 = (g & 1) ? a3 : a1;
      hp = pk_bf16(h0, h1);
      lp = pk_bf16(h0 - bf_lo(hp), h1 - bf_hi(hp));
    }
    f32x4 acc = {0.f, 0.f, 0.f, 0.f};
    acc = mfma(buildA(hp, 0), wov[32], acc);
    acc = mfma(buildA(hp, 1), wov[33], acc);
    acc = mfma(buildA(lp, 0), wov[32], acc);
    acc = mfma(buildA(lp, 1), wov[33], acc);
    STAMP(7);
    await_a(C_CTX, 4);
    STAMP(8);
    {
      const int b = r >> 3;
      const float gb = b ? gate1 : gate0;
      const int off = L_CTX + b * 512 + (32 * g + 4 * jj) * 4;
      const f32x4 cs = ((LDF4(off) + LDF4(off + 1024)) + (LDF4(off + 2048) + LDF4(off + 3072))) * gb;
      *(LAS v2u*)(L + L_PK + w * 512 + b * 256 + (16 * g + 2 * jj) * 4) = v2u{pk_bf16(cs.x, cs.y), pk_bf16(cs.z, cs.w)};
      FENCE();
      v2u pk[8];
#pragma unroll
      for (int k = 0; k < 8; ++k) pk[k] = *(const LAS v2u*)(L + L_PK + w * 512 + b * 256 + (16 * g + ((2 * k + 2 * j) & 15)) * 4);
#pragma unroll
      for (int i = 0; i < 32; ++i) acc = mfma(buildA(pk[i >> 2][(i >> 1) & 1], i & 1), wov[i], acc);
    }
    const unsigned cons = (g >> 1) * 128 + 32 * w + 2 * r + (g & 1);
    __builtin_amdgcn_raw_buffer_store_b128(tag4(acc, tag), rs, W_H4 + bid * 4096 + cons * 16, 0, 16);
    STAMP(9);
    STAMP_FLUSH();
  }
}
}

extern "C" void kernel_launch(void* const* d_in, const int* in_sizes, int n_in, void* d_out, int out_size, void* d_ws, size_t ws_size,
                              hipStream_t stream) {
  (void)in_sizes; (void)n_in; (void)out_size; (void)ws_size;
  (void)hipFuncSetAttribute((const void*)mk, hipFuncAttributeMaxDynamicSharedMemorySize, L_ALLOC);
  mk<<<256, 512, L_ALLOC, stream>>>((const u16*)d_in[0], (const u16*)d_in[1], (const u16*)d_in[2], (const u16*)d_in[3], (const u16*)d_in[4],
                                  (const u16*)d_in[5], (const u16*)d_in[7], (const u16*)d_in[8], (const u16*)d_in[9], (const u16*)d_in[10],
                                  (u16*)d_out, (unsigned char*)d_ws);
}
```

```cpp
#include <hip/hip_runtime.h>
#include <hip/hip_bf16.h>
#include <stdint.h>

typedef unsigned short u16;
typedef unsigned v4u __attribute__((ext_vector_type(4)));
typedef unsigned v2u __attribute__((ext_vector_type(2)));
typedef float f32x4 __attribute__((ext_vector_type(4)));
typedef short s16x4 __attribute__((ext_vector_type(4)));
typedef __bf16 bf16x8 __attribute__((ext_vector_type(8)));
#define LAS __attribute__((address_space(3)))

#define CODE_KB 22
#define H3_DELAY 32
#define H4_DELAY 40
#define H3_SLEEP 6
#define PB_S2 0

namespace {
constexpr int PITCH = 1056;
constexpr int L_KV = 0;
constexpr int L_Q = 128 * PITCH;
constexpr int L_P = L_Q + 16 * PITCH;
constexpr int L_SM = L_P + 4096;
constexpr int L_CNT = L_SM + 512;
constexpr int L_END = L_CNT + 64;
constexpr int L_OL = L_Q;
constexpr int L_CTX = L_OL + 1024;
constexpr int L_PK = L_CTX + 4096;
constexpr int L_FIN = L_PK + 2048;
static_assert(L_END <= 160 * 1024 - 512, "LDS");
enum { C_S1 = 0, C_S2, C_KV, C_S3, C_S4, C_S5, C_CTX, C_ST, C_FIN, C_GI };

constexpr unsigned W_EPOCH = 0;
constexpr unsigned W_H2 = 4096;
constexpr unsigned W_HF = W_H2 + 2 * 2 * 32 * 512 * 4;
constexpr unsigned W_H4 = W_HF + 224 * 2048;
constexpr unsigned W_H3 = W_H4 + 256 * 4096;
constexpr unsigned W_DBG = W_H3 + 256 * 32768;
constexpr unsigned W_END = W_DBG + 256 * 2 * 32 * 8;
constexpr unsigned OOB = 0xfffffff0u;
constexpr unsigned TMASK = 0xfffu;
constexpr int SPIN_CAP = 1 << 15;
constexpr int LSPIN_CAP = 1 << 22;

__device__ __forceinline__ f32x4 mfma(v4u a, v4u b, f32x4 c) {
  return __builtin_amdgcn_mfma_f32_16x16x32_bf16(__builtin_bit_cast(bf16x8, a), __builtin_bit_cast(bf16x8, b), c, 0, 0, 0);
}
__device__ __forceinline__ float bf_lo(unsigned w) { return __uint_as_float(w << 16); }
__device__ __forceinline__ float bf_hi(unsigned w) { return __uint_as_float(w & 0xffff0000u); }
__device__ __forceinline__ unsigned pk_bf16(float a, float b) {
  typedef __bf16 bf16x2 __attribute__((ext_vector_type(2)));
  typedef float f32x2 __attribute__((ext_vector_type(2)));
  f32x2 v = {a, b};
  return __builtin_bit_cast(unsigned, __builtin_convertvector(v, bf16x2));
}
__device__ __forceinline__ unsigned tagw(float v, unsigned tag) { return ((__float_as_uint(v) + 0x800u) & ~TMASK) | tag; }
__device__ __forceinline__ float untag(unsigned w) { return __uint_as_float(w & ~TMASK); }
__device__ __forceinline__ v4u tag4(f32x4 v, unsigned tag) { return v4u{tagw(v.x, tag), tagw(v.y, tag), tagw(v.z, tag), tagw(v.w, tag)}; }
__device__ __forceinline__ unsigned bad4(v4u w, unsigned tag) { return ((w.x ^ tag) | (w.y ^ tag) | (w.z ^ tag) | (w.w ^ tag)) & TMASK; }
template <int N> __device__ __forceinline__ float dpp_ror(float v) {
  return __int_as_float(__builtin_amdgcn_update_dpp(0, __float_as_int(v), 0x120 + N, 0xf, 0xf, false));
}
template <int N> __device__ __forceinline__ unsigned dpp_roru(unsigned v) { return (unsigned)__builtin_amdgcn_update_dpp(0, (int)v, 0x120 + N, 0xf, 0xf, false); }
__device__ __forceinline__ float x16_add(float v) { auto p = __builtin_amdgcn_permlane16_swap(__float_as_uint(v), __float_as_uint(v), false, false); return __uint_as_float(p[0]) + __uint_as_float(p[1]); }
__device__ __forceinline__ float x32_add(float v) { auto p = __builtin_amdgcn_permlane32_swap(__float_as_uint(v), __float_as_uint(v), false, false); return __uint_as_float(p[0]) + __uint_as_float(p[1]); }
__device__ __forceinline__ float x16_max(float v) { auto p = __builtin_amdgcn_permlane16_swap(__float_as_uint(v), __float_as_uint(v), false, false); return fmaxf(__uint_as_float(p[0]), __uint_as_float(p[1])); }
__device__ __forceinline__ float x32_max(float v) { auto p = __builtin_amdgcn_permlane32_swap(__float_as_uint(v), __float_as_uint(v), false, false); return fmaxf(__uint_as_float(p[0]), __uint_as_float(p[1])); }
__device__ __forceinline__ float wave_sum(float v) { v += dpp_ror<8>(v); v += dpp_ror<4>(v); v += dpp_ror<2>(v); v += dpp_ror<1>(v); return x32_add(x16_add(v)); }
__device__ __forceinline__ v4u ldw(const u16* p) { return *(const v4u*)p; }

#define LD128(off) (*(const LAS v4u*)(L + (off)))
#define LDF4(off) (*(const LAS f32x4*)(L + (off)))
#define FENCE() do { __builtin_amdgcn_sched_barrier(0); asm volatile("" ::: "memory"); } while (0)
#define LDH(off) __builtin_amdgcn_raw_buffer_load_b128(rs, (off), 0, 16)
#define STH(v, off) __builtin_amdgcn_raw_buffer_store_b128((v), rs, (off), 0, 16)

#define STAMP(i)
#define STAMP_FLUSH()
#define L_ALLOC L_END

#define GATHER(N, v, base, stride)                                                                 \
  {                                                                                                \
    unsigned pend_ = (1u << (N)) - 1u;                                                             \
    for (int spin_ = 0; spin_ < SPIN_CAP; ++spin_) {                                               \
      asm volatile("" ::: "memory");                                                               \
      v4u t_[N];                                                                                   \
      _Pragma("unroll") for (int i_ = 0; i_ < (N); ++i_) t_[i_] = LDH(((pend_ >> i_) & 1u) ? (base) + i_ * (stride) : OOB); \
      _Pragma("unroll") for (int i_ = 0; i_ < (N); ++i_) {                                         \
        const bool p_ = (pend_ >> i_) & 1u;                                                        \
        v[i_].x = p_ ? t_[i_].x : v[i_].x; v[i_].y = p_ ? t_[i_].y : v[i_].y;                      \
        v[i_].z = p_ ? t_[i_].z : v[i_].z; v[i_].w = p_ ? t_[i_].w : v[i_].w;                      \
        if (bad4(v[i_], tag) == 0) pend_ &= ~(1u << i_);                                           \
      }                                                                                            \
      if (__all(pend_ == 0)) break;                                                                \
      __builtin_amdgcn_s_sleep(1);                                                                 \
    }                                                                                              \
  }

__global__ __launch_bounds__(512, 2) void mk(const u16* __restrict__ x, const u16* __restrict__ ckv, const u16* __restrict__ wq,
                                             const u16* __restrict__ wuk, const u16* __restrict__ wuv, const u16* __restrict__ wo,
                                             const u16* __restrict__ wg, const u16* __restrict__ wu, const u16* __restrict__ wd,
                                             const u16* __restrict__ gw, u16* __restrict__ out, unsigned char* ws) {
  unsigned long long pc0;
  asm volatile("s_getpc_b64 %0" : "=s"(pc0));
  extern __shared__ __attribute__((aligned(16))) unsigned char lds[];
  LAS unsigned char* L = (LAS unsigned char*)lds;
  const int tid = threadIdx.x, lane = tid & 63, wave = __builtin_amdgcn_readfirstlane(tid >> 6);
  const int bid = blockIdx.x, xcd = bid & 7, slot = bid >> 3;
  const int h = xcd * 4 + (slot >> 3), j = slot & 7;
  const int ab = xcd >> 2, as = (xcd & 3) * 16 + (slot >> 1), hg = slot & 1;
  const int g = lane >> 4, r = lane & 15, jj = r & 7;
  const auto rs = __builtin_amdgcn_make_buffer_rsrc(ws, 0, W_END, 0x00020000);
  STAMP(0);
  int vzero = 0;
  asm volatile("" : "+v"(vzero));
  unsigned selE[4], selO[4];
#pragma unroll
  for (int k = 0; k < 4; ++k) {
    selE[k] = (k == (jj >> 1)) ? ((jj & 1) ? 0x01000c0cu : 0x0c0c0100u) : 0x0c0c0c0cu;
    selO[k] = (k == (jj >> 1)) ? ((jj & 1) ? 0x03020c0cu : 0x0c0c0302u) : 0x0c0c0c0cu;
  }
  auto buildA = [&](unsigned pw, int odd) -> v4u {
    v4u a;
    if (odd) { a.x = __builtin_amdgcn_perm(pw, pw, selO[0]); a.y = __builtin_amdgcn_perm(pw, pw, selO[1]); a.z = __builtin_amdgcn_perm(pw, pw, selO[2]); a.w = __builtin_amdgcn_perm(pw, pw, selO[3]); }
    else     { a.x = __builtin_amdgcn_perm(pw, pw, selE[0]); a.y = __builtin_amdgcn_perm(pw, pw, selE[1]); a.z = __builtin_amdgcn_perm(pw, pw, selE[2]); a.w = __builtin_amdgcn_perm(pw, pw, selE[3]); }
    return a;
  };
  auto arrive = [&](int c) {
    FENCE();
    if (lane == 0) asm volatile("ds_add_u32 %0, %1" ::"v"((unsigned)(L_CNT + 4 * c)), "v"(1u) : "memory");
    FENCE();
  };
  auto await = [&](int c, unsigned n) {
    FENCE();
    for (int s = 0; s < LSPIN_CAP; ++s) {
      unsigned v;
      asm volatile("ds_read_b32 %0, %1\n\ts_waitcnt lgkmcnt(0)" : "=v"(v) : "v"((unsigned)(L_CNT + 4 * c)) : "memory");
      if ((unsigned)__builtin_amdgcn_readfirstlane((int)v) >= n) break;
    }
    FENCE();
  };

  if (wave < 4) {
    const int w = wave;
    unsigned tag, epoch;
    {
      const int dq = j & 1, cq = j >> 1;
      const u16* wkb = wuk + ((size_t)(128 * cq + 32 * w + r) * 32 + h) * 128 + 64 * dq + 8 * g;
      v4u wk[4];
#pragma unroll
      for (int f = 0; f < 4; ++f) wk[f] = ldw(wkb + (size_t)(f >> 1) * (16 * 4096) + 32 * (f & 1));
      const u16* xp = x + (r >> 3) * 512 + 128 * w + 32 * g;
      v2u xv[8];
#pragma unroll
      for (int k = 0; k < 8; ++k) xv[k] = *(const v2u*)(xp + 16 * (k >> 2) + ((4 * k + 4 * cq) & 15));
      const u16* wqb = wq + (size_t)(128 * w + 32 * g + 16 * (r >> 3)) * 4096 + h * 128 + 64 * dq + 8 * (r & 7);
      v4u wv[16];
#pragma unroll
      for (int i = 0; i < 16; ++i) wv[i] = ldw(wqb + (size_t)((i + 4 * cq) & 15) * 4096);
      epoch = __builtin_amdgcn_raw_buffer_load_b32(rs, vzero + W_EPOCH, 0, 16);
      FENCE();
      __builtin_amdgcn_s_barrier();
      FENCE();
      f32x4 acc = {0.f, 0.f, 0.f, 0.f}, acc2 = {0.f, 0.f, 0.f, 0.f};
#pragma unroll
      for (int i = 0; i < 16; ++i) {
        acc = mfma(buildA(xv[i >> 2][(i >> 1) & 1], i & 1), wv[i], acc);
        acc2 = mfma(buildA(xv[4 + (i >> 2)][(i >> 1) & 1], i & 1), wv[i], acc2);
      }
#pragma unroll
      for (int e = 0; e < 4; ++e) { float v = (r < 8) ? acc[e] : acc2[e]; acc[e] = v + dpp_ror<8>(v); }
      STAMP(1);
      if (r < 8) {
        const unsigned h01 = pk_bf16(acc.x, acc.y), h23 = pk_bf16(acc.z, acc.w);
        const unsigned l01 = pk_bf16(acc.x - bf_lo(h01), acc.y - bf_hi(h01)), l23 = pk_bf16(acc.z - bf_lo(h23), acc.w - bf_hi(h23));
        const int b = g >> 1, d0 = 8 * r + 4 * (g & 1);
        *(LAS v2u*)(L + L_P + (b + 4 * w) * 160 + d0 * 2) = v2u{h01, h23};
        *(LAS v2u*)(L + L_P + (b + 2 + 4 * w) * 160 + d0 * 2) = v2u{l01, l23};
      }
      arrive(C_S1);
      await(C_S1, 4);
      const v4u qb0 = LD128(L_P + r * 160 + g * 16), qb1 = LD128(L_P + r * 160 + 64 + g * 16);
      f32x4 qa = {0.f, 0.f, 0.f, 0.f}, qc = {0.f, 0.f, 0.f, 0.f};
      qa = mfma(wk[0], qb0, qa); qc = mfma(wk[2], qb0, qc);
      qa = mfma(wk[1], qb1, qa); qc = mfma(wk[3], qb1, qc);
#pragma unroll
      for (int e = 0; e < 4; ++e) {
        float v = qa[e]; v += dpp_ror<8>(v); v += dpp_ror<4>(v); v += dpp_ror<2>(v);
        float u = qc[e]; u += dpp_ror<8>(u); u += dpp_ror<4>(u); u += dpp_ror<2>(u);
        qa[e] = ((r & 2) ? u : v) * (0.08838834764831845f * 1.4426950408889634f);
      }
      tag = (epoch + 1u) & TMASK;
      if (r < 4) STH(tag4(qa, tag), W_H2 + (((((r & 1) * 2 + (h >> 4)) * 2 + dq) * 16 + (h & 15)) * 512 + 128 * cq + 32 * w + 16 * (r >> 1) + 4 * g) * 4);
    }
    STAMP(2);
    await(C_KV, 4);
    STAMP(3);
    v4u kf[32];
    {
      v4u v[16];
      const unsigned base = W_H2 + (unsigned)(ab * 2 + hg) * 65536u + (unsigned)tid * 16u;
#pragma unroll
      for (int i = 0; i < 16; ++i) v[i] = LDH(base + i * 4096u);
      arrive(C_GI);
#pragma unroll
      for (int ks = 0; ks < 16; ++ks) {
        kf[2 * ks] = LD128(L_KV + (32 * w + r) * PITCH + ks * 64 + g * 16);
        kf[2 * ks + 1] = LD128(L_KV + (32 * w + 16 + r) * PITCH + ks * 64 + g * 16);
      }
      FENCE();
      unsigned bad0 = 0;
#pragma unroll
      for (int i = 0; i < 16; ++i) bad0 |= bad4(v[i], tag);
      if (!__all(bad0 == 0)) {
        for (int spin = 0; spin < SPIN_CAP; ++spin) {
          asm volatile("" ::: "memory");
#pragma unroll
          for (int i = 0; i < 16; ++i) v[i] = LDH(base + i * 4096u);
          unsigned bad = 0;
#pragma unroll
          for (int i = 0; i < 16; ++i) bad |= bad4(v[i], tag);
          if (__all(bad == 0)) break;
        }
        FENCE();
#pragma unroll
        for (int ks = 0; ks < 16; ++ks) {
          kf[2 * ks] = LD128(L_KV + (32 * w + r) * PITCH + ks * 64 + g * 16);
          kf[2 * ks + 1] = LD128(L_KV + (32 * w + 16 + r) * PITCH + ks * 64 + g * 16);
        }
      }
#pragma unroll
      for (int i = 0; i < 8; ++i) {
        const int head = 2 * i + (tid >> 7), c = 4 * (tid & 127);
        *(LAS v2u*)(L + L_Q + head * PITCH + c * 2) =
            v2u{pk_bf16(untag(v[i].x) + untag(v[i + 8].x), untag(v[i].y) + untag(v[i + 8].y)), pk_bf16(untag(v[i].z) + untag(v[i + 8].z), untag(v[i].w) + untag(v[i + 8].w))};
      }
    }
    STAMP(11);
    arrive(C_S2);
    v4u wvv[4];
    {
      const u16* wvb = wuv + ((size_t)(64 * j + 16 * w + 4 * g) * 32 + h) * 128 + 8 * r;
#pragma unroll
      for (int i = 0; i < 4; ++i) wvv[i] = ldw(wvb + (size_t)i * 4096);
    }
    await(C_S2, 4);
    STAMP(4);
    {
      f32x4 s0 = {0.f, 0.f, 0.f, 0.f}, s1 = {0.f, 0.f, 0.f, 0.f};
      const int tq = r >> 2, tp = r & 3;
      v4u vf[32];
      {
        v4u qf[16];
#pragma unroll
        for (int ks = 0; ks < 16; ++ks) qf[ks] = LD128(L_Q + r * PITCH + ks * 64 + g * 16);
        FENCE();
#pragma unroll
        for (int ks = 0; ks < 16; ++ks) {
          s0 = mfma(kf[2 * ks], qf[ks], s0);
          s1 = mfma(kf[2 * ks + 1], qf[ks], s1);
#pragma unroll
          for (int e = 0; e < 2; ++e) {
            const int ct = ks >> 1, i = 2 * (ks & 1) + e;
            const int a0 = L_KV + (32 * i + 4 * g + tq) * PITCH + (128 * w + 16 * ct + 4 * tp) * 2;
            const v2u t0 = __builtin_bit_cast(v2u, __builtin_amdgcn_ds_read_tr16_b64_v4i16((LAS s16x4*)(L + a0)));
            const v2u t1 = __builtin_bit_cast(v2u, __builtin_amdgcn_ds_read_tr16_b64_v4i16((LAS s16x4*)(L + a0 + 16 * PITCH)));
            vf[4 * ct + i] = v4u{t0.x, t0.y, t1.x, t1.y};
          }
          __builtin_amdgcn_sched_barrier(0);
        }
      }
      FENCE();
      float mx = fmaxf(fmaxf(fmaxf(s0.x, s0.y), fmaxf(s0.z, s0.w)), fmaxf(fmaxf(s1.x, s1.y), fmaxf(s1.z, s1.w)));
      mx = x32_max(x16_max(mx));
      if (g == 0) *(LAS float*)(L + L_SM + (r * 4 + w) * 4) = mx;
      arrive(C_S3);
      await(C_S3, 4);
      const f32x4 m0 = LDF4(L_SM + r * 16);
      const float M = fmaxf(fmaxf(m0.x, m0.y), fmaxf(m0.z, m0.w));
      f32x4 p0, p1;
#pragma unroll
      for (int e = 0; e < 4; ++e) { p0[e] = __builtin_amdgcn_exp2f(s0[e] - M); p1[e] = __builtin_amdgcn_exp2f(s1[e] - M); }
      *(LAS v4u*)(L + L_P + w * 1024 + lane * 16) = v4u{pk_bf16(p0.x, p0.y), pk_bf16(p0.z, p0.w), pk_bf16(p1.x, p1.y), pk_bf16(p1.z, p1.w)};
      float ls = ((p0.x + p0.y) + (p0.z + p0.w)) + ((p1.x + p1.y) + (p1.z + p1.w));
      ls = x32_add(x16_add(ls));
      if (g == 0) *(LAS float*)(L + L_SM + 256 + (r * 4 + w) * 4) = ls;
      arrive(C_S4);
      await(C_S4, 4);
      STAMP(5);
      v4u pf[4];
#pragma unroll
      for (int i = 0; i < 4; ++i) pf[i] = LD128(L_P + i * 1024 + lane * 16);
      unsigned lsb;
      float osc;
      {
        const f32x4 l0 = LDF4(L_SM + 256 + r * 16);
        const float lse = M + __builtin_amdgcn_logf((l0.x + l0.y) + (l0.z + l0.w));
        lsb = (__float_as_uint(lse) + 0x80u) & 0xffffff00u;
        osc = __builtin_amdgcn_exp2f(M - __uint_as_float(lsb));
      }
      const unsigned f1 = lsb >> 20, f3 = (lsb >> 8) & TMASK;
      const int stg = L_KV + w * (32 * 272);
      const unsigned h3base = W_H3 + (((ab * 64 + as) * 2 + hg) * 32 + 8 * w) * 1024 + lane * 16;
      f32x4 oc = {0.f, 0.f, 0.f, 0.f};
#pragma unroll
      for (int i = 0; i < 4; ++i) oc = mfma(vf[i], pf[i], oc);
      __builtin_amdgcn_sched_barrier(0);
#pragma unroll
      for (int ct = 0; ct < 8; ++ct) {
        f32x4 on = {0.f, 0.f, 0.f, 0.f};
        if (ct < 7) {
#pragma unroll
          for (int i = 0; i < 4; ++i) on = mfma(vf[4 * ct + 4 + i], pf[i], on);
        }
        const f32x4 o = oc * osc;
        *(LAS v4u*)(L + stg + ((ct >> 2) * 16 + r) * 272 + ((ct & 3) * 4 + g) * 16) = v4u{tagw(o.x, tag), tagw(o.y, f1), tagw(o.z, tag), tagw(o.w, f3)};
        if (ct >= 4) {
          const v4u d = LD128(stg + (4 * (ct - 4) + g) * 272 + r * 16);
          STH(d, h3base + (ct - 4) * 1024);
        }
        oc = on;
        FENCE();
      }
#pragma unroll
      for (int k = 4; k < 8; ++k) {
        const v4u d = LD128(stg + (4 * k + g) * 272 + r * 16);
        STH(d, h3base + k * 1024);
      }
    }
    arrive(C_ST);
    STAMP(6);
    __builtin_amdgcn_s_sleep(H3_DELAY);
    {
      const int b = w >> 1;
      const int c4 = tid & 15, sg = (tid >> 4) & 7;
      v4u v[8] = {};
      const unsigned obase = W_H3 + (unsigned)(((b * 64 + sg) * 2 + (h >> 4)) * 32 + 4 * j) * 1024u + (h & 15) * 256u + c4 * 16u;
      {
        unsigned pend = 0xffu;
        for (int spin = 0; spin < SPIN_CAP; ++spin) {
          asm volatile("" ::: "memory");
          v4u t[8];
#pragma unroll
          for (int i = 0; i < 8; ++i) t[i] = LDH(((pend >> i) & 1u) ? obase + i * (8u * 65536u) : OOB);
          unsigned badm = 0;
#pragma unroll
          for (int i = 0; i < 8; ++i) badm |= ((((t[i].x ^ tag) | (t[i].z ^ tag)) & TMASK) ? 1u : 0u) << i;
          badm |= dpp_roru<8>(badm); badm |= dpp_roru<4>(badm); badm |= dpp_roru<2>(badm); badm |= dpp_roru<1>(badm);
#pragma unroll
          for (int i = 0; i < 8; ++i) {
            const bool p = (pend >> i) & 1u;
            v[i].x = p ? t[i].x : v[i].x; v[i].y = p ? t[i].y : v[i].y; v[i].z = p ? t[i].z : v[i].z; v[i].w = p ? t[i].w : v[i].w;
          }
          pend &= badm;
          if (__all(pend == 0)) break;
          __builtin_amdgcn_s_sleep(H3_SLEEP);
        }
      }
      STAMP(7);
      float ls[8];
      float Mw = -3.0e38f;
#pragma unroll
      for (int i = 0; i < 8; ++i) { ls[i] = __uint_as_float(((v[i].y & TMASK) << 20) | ((v[i].w & TMASK) << 8)); Mw = fmaxf(Mw, ls[i]); }
      Mw = x32_max(x16_max(Mw));
      f32x4 o4 = {0.f, 0.f, 0.f, 0.f};
      float Lw = 0.f;
#pragma unroll
      for (int i = 0; i < 8; ++i) {
        const float ws_ = __builtin_amdgcn_exp2f(ls[i] - Mw);
        Lw += ws_;
        o4.x += ws_ * untag(v[i].x); o4.y += ws_ * untag(v[i].y); o4.z += ws_ * untag(v[i].z); o4.w += ws_ * untag(v[i].w);
      }
#pragma unroll
      for (int e2 = 0; e2 < 4; ++e2) o4[e2] = x32_add(x16_add(o4[e2]));
      Lw = x32_add(x16_add(Lw));
      if (g == 0) *(LAS f32x4*)(L + L_OL + w * 256 + c4 * 16) = o4;
      if (lane == 0) *(LAS v2u*)(L + L_SM + w * 8) = v2u{__float_as_uint(Mw), __float_as_uint(Lw)};
    }
    arrive(C_S5);
    await(C_S5, 4);
    {
      const int b = r >> 3;
      const f32x4 oa = LDF4(L_OL + (2 * b) * 256 + (16 * w + 4 * g) * 4), ob = LDF4(L_OL + (2 * b + 1) * 256 + (16 * w + 4 * g) * 4);
      const f32x4 ml = LDF4(L_SM + b * 16);
      const float Mb = fmaxf(ml.x, ml.z);
      const float ea = __builtin_amdgcn_exp2f(ml.x - Mb), eb = __builtin_amdgcn_exp2f(ml.z - Mb);
      const float inv = 1.f / (ml.y * ea + ml.w * eb);
      const f32x4 ov = oa * (ea * inv) + ob * (eb * inv);
      const unsigned h01 = pk_bf16(ov.x, ov.y), h23 = pk_bf16(ov.z, ov.w);
      const unsigned l01 = pk_bf16(ov.x - bf_lo(h01), ov.y - bf_hi(h01)), l23 = pk_bf16(ov.z - bf_lo(h23), ov.w - bf_hi(h23));
      f32x4 c0 = {0.f, 0.f, 0.f, 0.f}, c1 = {0.f, 0.f, 0.f, 0.f};
      c0 = mfma(buildA(h01, 0), wvv[0], c0); c1 = mfma(buildA(l01, 0), wvv[0], c1);
      c0 = mfma(buildA(h01, 1), wvv[1], c0); c1 = mfma(buildA(l01, 1), wvv[1], c1);
      c0 = mfma(buildA(h23, 0), wvv[2], c0); c1 = mfma(buildA(l23, 0), wvv[2], c1);
      c0 = mfma(buildA(h23, 1), wvv[3], c0); c1 = mfma(buildA(l23, 1), wvv[3], c1);
      *(LAS f32x4*)(L + L_CTX + w * 1024 + (g >> 1) * 512 + (8 * r + 4 * (g & 1)) * 4) = c0 + c1;
    }
    arrive(C_CTX);
    STAMP(8);
    if (xcd == (slot & 7)) {
      v4u q[8] = {};
      const unsigned qbase = W_H4 + (unsigned)(64 * w + (lane >> 3)) * 4096u + slot * 128u + (lane & 7) * 16u;
      __builtin_amdgcn_s_sleep(H4_DELAY);
      GATHER(8, q, qbase, 8u * 4096u);
      STAMP(9);
      f32x4 t4;
#pragma unroll
      for (int e2 = 0; e2 < 4; ++e2) {
        float t = ((untag(q[0][e2]) + untag(q[1][e2])) + (untag(q[2][e2]) + untag(q[3][e2]))) + ((untag(q[4][e2]) + untag(q[5][e2])) + (untag(q[6][e2]) + untag(q[7][e2])));
        t += dpp_ror<8>(t);
        t4[e2] = x32_add(x16_add(t));
      }
      if (lane < 8) *(LAS f32x4*)(L + L_FIN + w * 128 + lane * 16) = t4;
      arrive(C_FIN);
      if (w == 0) {
        await(C_FIN, 4);
        if (lane < 8) {
          const f32x4 o = (LDF4(L_FIN + lane * 16) + LDF4(L_FIN + 128 + lane * 16)) + (LDF4(L_FIN + 256 + lane * 16) + LDF4(L_FIN + 384 + lane * 16));
          *(v2u*)(out + slot * 32 + lane * 4) = v2u{pk_bf16(o.x, o.y), pk_bf16(o.z, o.w)};
          if (bid == 0 && lane == 0) __builtin_amdgcn_raw_buffer_store_b32(epoch + 1u, rs, W_EPOCH, 0, 16);
        }
      }
    }
    STAMP(10);
    STAMP_FLUSH();
  } else {
    const int w = wave - 4;
    if (w == 0 && lane < 16) *(LAS unsigned*)(L + L_CNT + 4 * lane) = 0u;
    if (w == 3 && slot < CODE_KB) {
      const v4u cv = *(const v4u*)((const unsigned char*)(pc0 & ~255ull) + slot * 1024 + lane * 16);
      asm volatile("" ::"v"(cv));
    }
    asm volatile("s_waitcnt lgkmcnt(0)" ::: "memory");
    FENCE();
    __builtin_amdgcn_s_barrier();
    FENCE();
    const u16* src = ckv + ((size_t)ab * 8192 + as * 128 + w * 32) * 512 + lane * 8;
#pragma unroll
    for (int i = 0; i < 32; ++i)
      __builtin_amdgcn_global_load_lds((const void*)(src + i * 512), (LAS void*)(L + L_KV + (w * 32 + i) * PITCH), 16, 0, 0);
    FENCE();
    __builtin_amdgcn_s_waitcnt(0x0f70);
    arrive(C_KV);
    STAMP(1);
    await(C_GI, 4);
    v4u wov[34];
    const u16* wob = wo + ((size_t)h * 128 + 32 * g) * 512 + 128 * w + 8 * r;
#pragma unroll
    for (int i = 0; i < 4; ++i) wov[i] = ldw(wob + ((i + 4 * j) & 31) * 512);
    const v4u gx0 = ldw(x + lane * 8), gx1 = ldw(x + 512 + lane * 8), gwv = ldw(gw + lane * 8);
    const unsigned epoch = __builtin_amdgcn_raw_buffer_load_b32(rs, vzero + W_EPOCH, 0, 16);
    {
      const int u = bid < 224 ? bid : 0;
      const u16* wdb = wd + (size_t)(8 * u + 2 * g) * 512 + 128 * w + 8 * r;
      wov[32] = ldw(wdb);
      wov[33] = ldw(wdb + 512);
    }
    v4u fv[8]; v4u fx;
    const bool ffn1 = (w < 2) && (bid < 224);
    const int cb = bid % 14, rb = bid / 14;
    if (ffn1) {
      const u16* fb = (w == 0 ? wg : wu) + (size_t)(32 * rb + 8 * g) * 1792 + 128 * cb + 8 * r;
#pragma unroll
      for (int i = 0; i < 8; ++i) fv[i] = ldw(fb + (size_t)i * 1792);
      fx = ldw(x + (r >> 3) * 512 + 32 * rb + 8 * g);
    }
    FENCE();
    STAMP(4);
#pragma unroll
    for (int i = 4; i < 32; ++i) wov[i] = ldw(wob + ((i + 4 * j) & 31) * 512);
    FENCE();
    STAMP(5);
    const unsigned tag = (epoch + 1u) & TMASK;
    if (ffn1) {
      f32x4 acc = {0.f, 0.f, 0.f, 0.f};
#pragma unroll
      for (int i = 0; i < 8; ++i) acc = mfma(buildA(fx[i >> 1], i & 1), fv[i], acc);
      STH(tag4(acc, tag), W_HF + (16 * cb + r) * 2048 + ((w * 16 + rb) * 4 + g) * 16);
    }
    float gate0, gate1;
    {
      float s0 = 0.f, s1 = 0.f;
#pragma unroll
      for (int k = 0; k < 4; ++k) {
        s0 += bf_lo(gx0[k]) * bf_lo(gwv[k]) + bf_hi(gx0[k]) * bf_hi(gwv[k]);
        s1 += bf_lo(gx1[k]) * bf_lo(gwv[k]) + bf_hi(gx1[k]) * bf_hi(gwv[k]);
      }
      s0 = wave_sum(s0); s1 = wave_sum(s1);
      gate0 = 1.f / (1.f + __expf(-s0));
      gate1 = 1.f / (1.f + __expf(-s1));
    }
    STAMP(2);
    await(C_ST, 4);
    __builtin_amdgcn_s_sleep(H3_DELAY);
    STAMP(6);
    unsigned hp = 0u, lp = 0u;
    if (bid < 224) {
      v4u ff[2] = {};
      const unsigned fbase = W_HF + bid * 2048 + lane * 16;
      GATHER(2, ff, fbase, 1024u);
      f32x4 hv;
      const float og = 1.f - (((lane >> 1) & 1) ? gate1 : gate0);
#pragma unroll
      for (int e2 = 0; e2 < 4; ++e2) {
        float t = untag(ff[0][e2]); t += dpp_ror<8>(t); t += dpp_ror<4>(t); t = x32_add(x16_add(t));
        float u = untag(ff[1][e2]); u += dpp_ror<8>(u); u += dpp_ror<4>(u); u = x32_add(x16_add(u));
        const float a2 = 2.f * 0.7978845608028654f * (t + 0.044715f * t * t * t);
        hv[e2] = t / (1.f + __expf(-a2)) * u * og;
      }
      const int srcl = (r >> 3) * 2 + (g >> 1);
      const float a0 = __shfl(hv.x, srcl), a1 = __shfl(hv.y, srcl), a2_ = __shfl(hv.z, srcl), a3 = __shfl(hv.w, srcl);
      const float h0 = (g & 1) ? a2_ : a0, h1 = (g & 1) ? a3 : a1;
      hp = pk_bf16(h0, h1);
      lp = pk_bf16(h0 - bf_lo(hp), h1 - bf_hi(hp));
    }
    f32x4 acc = {0.f, 0.f, 0.f, 0.f};
    acc = mfma(buildA(hp, 0), wov[32], acc);
    acc = mfma(buildA(hp, 1), wov[33], acc);
    acc = mfma(buildA(lp, 0), wov[32], acc);
    acc = mfma(buildA(lp, 1), wov[33], acc);
    STAMP(7);
    await(C_CTX, 4);
    STAMP(8);
    {
      const int b = r >> 3;
      const float gb = b ? gate1 : gate0;
      const int off = L_CTX + b * 512 + (32 * g + 4 * jj) * 4;
      const f32x4 cs = ((LDF4(off) + LDF4(off + 1024)) + (LDF4(off + 2048) + LDF4(off + 3072))) * gb;
      *(LAS v2u*)(L + L_PK + w * 512 + b * 256 + (16 * g + 2 * jj) * 4) = v2u{pk_bf16(cs.x, cs.y), pk_bf16(cs.z, cs.w)};
      FENCE();
      v2u pk[8];
#pragma unroll
      for (int k = 0; k < 8; ++k) pk[k] = *(const LAS v2u*)(L + L_PK + w * 512 + b * 256 + (16 * g + ((2 * k + 2 * j) & 15)) * 4);
#pragma unroll
      for (int i = 0; i < 32; ++i) acc = mfma(buildA(pk[i >> 2][(i >> 1) & 1], i & 1), wov[i], acc);
    }
    STH(tag4(acc, tag), W_H4 + bid * 4096 + ((g >> 1) * 128 + 32 * w + 2 * r + (g & 1)) * 16);
    STAMP(9);
    STAMP_FLUSH();
  }
}
}

extern "C" void kernel_launch(void* const* d_in, const int* in_sizes, int n_in, void* d_out, int out_size, void* d_ws, size_t ws_size,
                              hipStream_t stream) {
  (void)in_sizes; (void)n_in; (void)out_size; (void)ws_size;
  (void)hipFuncSetAttribute((const void*)mk, hipFuncAttributeMaxDynamicSharedMemorySize, L_ALLOC);
  mk<<<256, 512, L_ALLOC, stream>>>((const u16*)d_in[0], (const u16*)d_in[1], (const u16*)d_in[2], (const u16*)d_in[3], (const u16*)d_in[4],
                                    (const u16*)d_in[5], (const u16*)d_in[7], (const u16*)d_in[8], (const u16*)d_in[9], (const u16*)d_in[10],
                                    (u16*)d_out, (unsigned char*)d_ws);
}
```

```cpp
#include <hip/hip_runtime.h>
#include <hip/hip_bf16.h>
#include <stdint.h>

typedef unsigned short u16;
typedef unsigned v4u __attribute__((ext_vector_type(4)));
typedef unsigned v2u __attribute__((ext_vector_type(2)));
typedef float f32x4 __attribute__((ext_vector_type(4)));
typedef short s16x4 __attribute__((ext_vector_type(4)));
typedef __bf16 bf16x8 __attribute__((ext_vector_type(8)));
#define LAS __attribute__((address_space(3)))

namespace {
constexpr int CODE_KB = 23;
constexpr int DMA0 = 4;
constexpr int H2_DELAY = 10;
constexpr int H3_DELAY = 30;
constexpr int H4_DELAY = 48;
constexpr int H3_SLEEP = 6;
constexpr int PITCH = 1056;
constexpr int L_KV = 0;
constexpr int L_Q = 128 * PITCH;
constexpr int L_P = L_Q + 16 * PITCH;
constexpr int L_SM = L_P + 4096;
constexpr int L_CNT = L_SM + 512;
constexpr int L_KM = L_CNT + 64;
constexpr int L_END = L_KM + 16;
constexpr int L_OL = L_Q;
constexpr int L_CTX = L_OL + 1024;
constexpr int L_PK = L_CTX + 4096;
constexpr int L_FIN = L_PK + 2048;
static_assert(L_END <= 160 * 1024, "LDS");
enum { C_S1 = 0, C_S2, C_KV, C_S3, C_S4, C_S5, C_CTX, C_ST, C_FIN, C_GI, C_ISS, C_QI };

constexpr unsigned W_EPOCH = 0;
constexpr unsigned W_H2 = 4096;
constexpr unsigned W_HF = W_H2 + 2 * 2 * 32 * 512 * 4;
constexpr unsigned W_H4 = W_HF + 224 * 2048;
constexpr unsigned W_LS = W_H4 + 256 * 4096;
constexpr unsigned W_H3 = W_LS + 32 * 2 * 64 * 16;
constexpr unsigned W_END = W_H3 + 256 * 16384;
constexpr unsigned OOB = 0xfffffff0u;
constexpr unsigned TMASK = 0xfffu;
constexpr int SPIN_CAP = 1 << 15;
constexpr int LSPIN_CAP = 1 << 22;

__device__ __forceinline__ f32x4 mfma(v4u a, v4u b, f32x4 c) {
  return __builtin_amdgcn_mfma_f32_16x16x32_bf16(__builtin_bit_cast(bf16x8, a), __builtin_bit_cast(bf16x8, b), c, 0, 0, 0);
}
__device__ __forceinline__ float bf_lo(unsigned w) { return __uint_as_float(w << 16); }
__device__ __forceinline__ float bf_hi(unsigned w) { return __uint_as_float(w & 0xffff0000u); }
__device__ __forceinline__ unsigned pk_bf16(float a, float b) {
  typedef __bf16 bf16x2 __attribute__((ext_vector_type(2)));
  typedef float f32x2 __attribute__((ext_vector_type(2)));
  f32x2 v = {a, b};
  return __builtin_bit_cast(unsigned, __builtin_convertvector(v, bf16x2));
}
typedef _Float16 f16x2 __attribute__((ext_vector_type(2)));
__device__ __forceinline__ unsigned pk_f16(float a, float b) {
  typedef float f32x2 __attribute__((ext_vector_type(2)));
  f32x2 v = {a, b};
  return __builtin_bit_cast(unsigned, __builtin_convertvector(v, f16x2));
}
__device__ __forceinline__ unsigned tagw(float v, unsigned tag) { return ((__float_as_uint(v) + 0x800u) & ~TMASK) | tag; }
__device__ __forceinline__ float untag(unsigned w) { return __uint_as_float(w & ~TMASK); }
__device__ __forceinline__ v4u tag4(f32x4 v, unsigned tag) { return v4u{tagw(v.x, tag), tagw(v.y, tag), tagw(v.z, tag), tagw(v.w, tag)}; }
__device__ __forceinline__ unsigned bad4(v4u w, unsigned tag) { return ((w.x ^ tag) | (w.y ^ tag) | (w.z ^ tag) | (w.w ^ tag)) & TMASK; }
template <int N> __device__ __forceinline__ float dpp_ror(float v) {
  return __int_as_float(__builtin_amdgcn_update_dpp(0, __float_as_int(v), 0x120 + N, 0xf, 0xf, false));
}
template <int N> __device__ __forceinline__ unsigned dpp_roru(unsigned v) { return (unsigned)__builtin_amdgcn_update_dpp(0, (int)v, 0x120 + N, 0xf, 0xf, false); }
__device__ __forceinline__ float x16_add(float v) { auto p = __builtin_amdgcn_permlane16_swap(__float_as_uint(v), __float_as_uint(v), false, false); return __uint_as_float(p[0]) + __uint_as_float(p[1]); }
__device__ __forceinline__ float x32_add(float v) { auto p = __builtin_amdgcn_permlane32_swap(__float_as_uint(v), __float_as_uint(v), false, false); return __uint_as_float(p[0]) + __uint_as_float(p[1]); }
__device__ __forceinline__ float x16_max(float v) { auto p = __builtin_amdgcn_permlane16_swap(__float_as_uint(v), __float_as_uint(v), false, false); return fmaxf(__uint_as_float(p[0]), __uint_as_float(p[1])); }
__device__ __forceinline__ float x32_max(float v) { auto p = __builtin_amdgcn_permlane32_swap(__float_as_uint(v), __float_as_uint(v), false, false); return fmaxf(__uint_as_float(p[0]), __uint_as_float(p[1])); }
__device__ __forceinline__ float wave_sum(float v) { v += dpp_ror<8>(v); v += dpp_ror<4>(v); v += dpp_ror<2>(v); v += dpp_ror<1>(v); return x32_add(x16_add(v)); }
__device__ __forceinline__ v4u ldw(const u16* p) { return *(const v4u*)p; }

#define LD128(off) (*(const LAS v4u*)(L + (off)))
#define LDF4(off) (*(const LAS f32x4*)(L + (off)))
#define FENCE() do { __builtin_amdgcn_sched_barrier(0); asm volatile("" ::: "memory"); } while (0)
#define LDH(off) __builtin_amdgcn_raw_buffer_load_b128(rs, (off), 0, 16)
#define STH(v, off) __builtin_amdgcn_raw_buffer_store_b128((v), rs, (off), 0, 16)

#define GATHER(N, v, base, stride)                                                                 \
  {                                                                                                \
    unsigned pend_ = (1u << (N)) - 1u;                                                             \
    for (int spin_ = 0; spin_ < SPIN_CAP; ++spin_) {                                               \
      asm volatile("" ::: "memory");                                                               \
      v4u t_[N];                                                                                   \
      _Pragma("unroll") for (int i_ = 0; i_ < (N); ++i_) t_[i_] = LDH(((pend_ >> i_) & 1u) ? (base) + i_ * (stride) : OOB); \
      _Pragma("unroll") for (int i_ = 0; i_ < (N); ++i_) {                                         \
        const bool p_ = (pend_ >> i_) & 1u;                                                        \
        v[i_].x = p_ ? t_[i_].x : v[i_].x; v[i_].y = p_ ? t_[i_].y : v[i_].y;                      \
        v[i_].z = p_ ? t_[i_].z : v[i_].z; v[i_].w = p_ ? t_[i_].w : v[i_].w;                      \
        if (bad4(v[i_], tag) == 0) pend_ &= ~(1u << i_);                                           \
      }                                                                                            \
      if (__all(pend_ == 0)) break;                                                                \
      __builtin_amdgcn_s_sleep(1);                                                                 \
    }                                                                                              \
  }

__global__ __launch_bounds__(512, 2) void mk(const u16* __restrict__ x, const u16* __restrict__ ckv, const u16* __restrict__ wq,
                                             const u16* __restrict__ wuk, const u16* __restrict__ wuv, const u16* __restrict__ wo,
                                             const u16* __restrict__ wg, const u16* __restrict__ wu, const u16* __restrict__ wd,
                                             const u16* __restrict__ gw, u16* __restrict__ out, unsigned char* ws) {
  unsigned long long pc0;
  asm volatile("s_getpc_b64 %0" : "=s"(pc0));
  extern __shared__ __attribute__((aligned(16))) unsigned char lds[];
  LAS unsigned char* L = (LAS unsigned char*)lds;
  const int tid = threadIdx.x, lane = tid & 63, wave = __builtin_amdgcn_readfirstlane(tid >> 6);
  const int bid = blockIdx.x, xcd = bid & 7, slot = bid >> 3;
  const int h = xcd * 4 + (slot >> 3), j = slot & 7;
  const int ab = xcd >> 2, as = (xcd & 3) * 16 + (slot >> 1), hg = slot & 1;
  const int g = lane >> 4, r = lane & 15, jj = r & 7;
  const auto rs = __builtin_amdgcn_make_buffer_rsrc(ws, 0, W_END, 0x00020000);
  v4u cv = {0u, 0u, 0u, 0u};
  if (wave == 7 && slot < CODE_KB)
    cv = *(const __attribute__((address_space(1))) v4u*)((pc0 & ~255ull) + slot * 1024 + lane * 16);
  if (tid < 16) *(LAS unsigned*)(L + L_CNT + 4 * tid) = 0u;
  asm volatile("s_waitcnt lgkmcnt(0)" ::: "memory");
  __builtin_amdgcn_s_barrier();
  asm volatile("" ::: "memory");
  int vzero = 0;
  asm volatile("" : "+v"(vzero));
  unsigned selE[4], selO[4];
#pragma unroll
  for (int k = 0; k < 4; ++k) {
    selE[k] = (k == (jj >> 1)) ? ((jj & 1) ? 0x01000c0cu : 0x0c0c0100u) : 0x0c0c0c0cu;
    selO[k] = (k == (jj >> 1)) ? ((jj & 1) ? 0x03020c0cu : 0x0c0c0302u) : 0x0c0c0c0cu;
  }
  auto buildA = [&](unsigned pw, int odd) -> v4u {
    v4u a;
    if (odd) { a.x = __builtin_amdgcn_perm(pw, pw, selO[0]); a.y = __builtin_amdgcn_perm(pw, pw, selO[1]); a.z = __builtin_amdgcn_perm(pw, pw, selO[2]); a.w = __builtin_amdgcn_perm(pw, pw, selO[3]); }
    else     { a.x = __builtin_amdgcn_perm(pw, pw, selE[0]); a.y = __builtin_amdgcn_perm(pw, pw, selE[1]); a.z = __builtin_amdgcn_perm(pw, pw, selE[2]); a.w = __builtin_amdgcn_perm(pw, pw, selE[3]); }
    return a;
  };
  auto arrive = [&](int c) {
    FENCE();
    if (lane == 0) asm volatile("ds_add_u32 %0, %1" ::"v"((unsigned)(L_CNT + 4 * c)), "v"(1u) : "memory");
    FENCE();
  };
  auto await = [&](int c, unsigned n) {
    FENCE();
    for (int s = 0; s < LSPIN_CAP; ++s) {
      unsigned v;
      asm volatile("ds_read_b32 %0, %1\n\ts_waitcnt lgkmcnt(0)" : "=v"(v) : "v"((unsigned)(L_CNT + 4 * c)) : "memory");
      if ((unsigned)__builtin_amdgcn_readfirstlane((int)v) >= n) break;
    }
    FENCE();
  };

  if (__builtin_expect(wave < 4, 1)) {
    const int w = wave;
    unsigned tag, epoch;
    {
      const int dq = j & 1, cq = j >> 1;
      const u16* wkb = wuk + ((size_t)(128 * cq + 32 * w + r) * 32 + h) * 128 + 64 * dq + 8 * g;
      v4u wk[4];
#pragma unroll
      for (int f = 0; f < 4; ++f) wk[f] = ldw(wkb + (size_t)(f >> 1) * (16 * 4096) + 32 * (f & 1));
      const u16* xp = x + (r >> 3) * 512 + 128 * w + 32 * g;
      v2u xv[8];
#pragma unroll
      for (int k = 0; k < 8; ++k) xv[k] = *(const v2u*)(xp + 16 * (k >> 2) + ((4 * k + 4 * cq) & 15));
      const u16* wqb = wq + (size_t)(128 * w + 32 * g + 16 * (r >> 3)) * 4096 + h * 128 + 64 * dq + 8 * (r & 7);
      v4u wv[16];
#pragma unroll
      for (int i = 0; i < 16; ++i) wv[i] = ldw(wqb + (size_t)((i + 4 * cq) & 15) * 4096);
      epoch = __builtin_amdgcn_raw_buffer_load_b32(rs, vzero + W_EPOCH, 0, 16);
      arrive(C_QI);
      f32x4 acc = {0.f, 0.f, 0.f, 0.f}, acc2 = {0.f, 0.f, 0.f, 0.f};
#pragma unroll
      for (int i = 0; i < 16; ++i) {
        acc = mfma(buildA(xv[i >> 2][(i >> 1) & 1], i & 1), wv[i], acc);
        acc2 = mfma(buildA(xv[4 + (i >> 2)][(i >> 1) & 1], i & 1), wv[i], acc2);
      }
#pragma unroll
      for (int e = 0; e < 4; ++e) { float v = (r < 8) ? acc[e] : acc2[e]; acc[e] = v + dpp_ror<8>(v); }
      if (r < 8) {
        const unsigned h01 = pk_bf16(acc.x, acc.y), h23 = pk_bf16(acc.z, acc.w);
        const unsigned l01 = pk_bf16(acc.x - bf_lo(h01), acc.y - bf_hi(h01)), l23 = pk_bf16(acc.z - bf_lo(h23), acc.w - bf_hi(h23));
        const int b = g >> 1, d0 = 8 * r + 4 * (g & 1);
        *(LAS v2u*)(L + L_P + (b + 4 * w) * 160 + d0 * 2) = v2u{h01, h23};
        *(LAS v2u*)(L + L_P + (b + 2 + 4 * w) * 160 + d0 * 2) = v2u{l01, l23};
      }
      arrive(C_S1);
      await(C_S1, 4);
      const v4u qb0 = LD128(L_P + r * 160 + g * 16), qb1 = LD128(L_P + r * 160 + 64 + g * 16);
      f32x4 qa = {0.f, 0.f, 0.f, 0.f}, qc = {0.f, 0.f, 0.f, 0.f};
      qa = mfma(wk[0], qb0, qa); qc = mfma(wk[2], qb0, qc);
      qa = mfma(wk[1], qb1, qa); qc = mfma(wk[3], qb1, qc);
#pragma unroll
      for (int e = 0; e < 4; ++e) {
        float v = qa[e]; v += dpp_ror<8>(v); v += dpp_ror<4>(v); v += dpp_ror<2>(v);
        float u = qc[e]; u += dpp_ror<8>(u); u += dpp_ror<4>(u); u += dpp_ror<2>(u);
        qa[e] = ((r & 2) ? u : v) * (0.08838834764831845f * 1.4426950408889634f);
      }
      tag = (epoch + 1u) & TMASK;
      if (r < 4) STH(tag4(qa, tag), W_H2 + (((((r & 1) * 2 + (h >> 4)) * 2 + dq) * 16 + (h & 15)) * 512 + 128 * cq + 32 * w + 16 * (r >> 1) + 4 * g) * 4);
    }
    await(C_ISS, 4);
    __builtin_amdgcn_s_sleep(H2_DELAY);
    v4u kf[32];
    {
      v4u v[16];
      const unsigned base = W_H2 + (unsigned)(ab * 2 + hg) * 65536u + (unsigned)tid * 16u;
#pragma unroll
      for (int i = 0; i < 16; ++i) v[i] = LDH(base + i * 4096u);
      arrive(C_GI);
      await(C_KV, 4);
#pragma unroll
      for (int ks = 0; ks < 16; ++ks) {
        kf[2 * ks] = LD128(L_KV + (32 * w + r) * PITCH + ks * 64 + g * 16);
        kf[2 * ks + 1] = LD128(L_KV + (32 * w + 16 + r) * PITCH + ks * 64 + g * 16);
      }
      {
        f32x4 n0 = {0.f, 0.f, 0.f, 0.f}, n1 = {0.f, 0.f, 0.f, 0.f};
#pragma unroll
        for (int ks = 0; ks < 16; ++ks) { n0 = mfma(kf[2 * ks], kf[2 * ks], n0); n1 = mfma(kf[2 * ks + 1], kf[2 * ks + 1], n1); }
        float kx = fmaxf(fmaxf(fmaxf(n0.x, n0.y), fmaxf(n0.z, n0.w)), fmaxf(fmaxf(n1.x, n1.y), fmaxf(n1.z, n1.w)));
        kx = fmaxf(kx, dpp_ror<8>(kx)); kx = fmaxf(kx, dpp_ror<4>(kx)); kx = fmaxf(kx, dpp_ror<2>(kx)); kx = fmaxf(kx, dpp_ror<1>(kx));
        kx = x32_max(x16_max(kx));
        if (lane == 0) *(LAS float*)(L + L_KM + w * 4) = kx;
      }
      FENCE();
      unsigned bad0 = 0;
#pragma unroll
      for (int i = 0; i < 16; ++i) bad0 |= bad4(v[i], tag);
      if (!__all(bad0 == 0)) {
        for (int spin = 0; spin < SPIN_CAP; ++spin) {
          asm volatile("" ::: "memory");
#pragma unroll
          for (int i = 0; i < 16; ++i) v[i] = LDH(base + i * 4096u);
          unsigned bad = 0;
#pragma unroll
          for (int i = 0; i < 16; ++i) bad |= bad4(v[i], tag);
          if (__all(bad == 0)) break;
        }
        FENCE();
#pragma unroll
        for (int ks = 0; ks < 16; ++ks) {
          kf[2 * ks] = LD128(L_KV + (32 * w + r) * PITCH + ks * 64 + g * 16);
          kf[2 * ks + 1] = LD128(L_KV + (32 * w + 16 + r) * PITCH + ks * 64 + g * 16);
        }
      }
#pragma unroll
      for (int i = 0; i < 8; ++i) {
        const int head = 2 * i + (tid >> 7), c = 4 * (tid & 127);
        *(LAS v2u*)(L + L_Q + head * PITCH + c * 2) =
            v2u{pk_bf16(untag(v[i].x) + untag(v[i + 8].x), untag(v[i].y) + untag(v[i + 8].y)), pk_bf16(untag(v[i].z) + untag(v[i + 8].z), untag(v[i].w) + untag(v[i + 8].w))};
      }
    }
    arrive(C_S2);
    v4u wvv[4];
    {
      const u16* wvb = wuv + ((size_t)(64 * j + 16 * w + 4 * g) * 32 + h) * 128 + 8 * r;
#pragma unroll
      for (int i = 0; i < 4; ++i) wvv[i] = ldw(wvb + (size_t)i * 4096);
    }
    await(C_S2, 4);
    {
      f32x4 s0 = {0.f, 0.f, 0.f, 0.f}, s1 = {0.f, 0.f, 0.f, 0.f};
      const int tq = r >> 2, tp = r & 3;
      v4u vf[32];
      {
        v4u qf[16];
#pragma unroll
        for (int ks = 0; ks < 16; ++ks) qf[ks] = LD128(L_Q + r * PITCH + ks * 64 + g * 16);
        FENCE();
#pragma unroll
        for (int ks = 0; ks < 16; ++ks) {
          s0 = mfma(kf[2 * ks], qf[ks], s0);
          s1 = mfma(kf[2 * ks + 1], qf[ks], s1);
#pragma unroll
          for (int e = 0; e < 2; ++e) {
            const int ct = ks >> 1, i = 2 * (ks & 1) + e;
            const int a0 = L_KV + (32 * i + 4 * g + tq) * PITCH + (128 * w + 16 * ct + 4 * tp) * 2;
            const v2u t0 = __builtin_bit_cast(v2u, __builtin_amdgcn_ds_read_tr16_b64_v4i16((LAS s16x4*)(L + a0)));
            const v2u t1 = __builtin_bit_cast(v2u, __builtin_amdgcn_ds_read_tr16_b64_v4i16((LAS s16x4*)(L + a0 + 16 * PITCH)));
            vf[4 * ct + i] = v4u{t0.x, t0.y, t1.x, t1.y};
          }
          __builtin_amdgcn_sched_barrier(0);
        }
      }
      FENCE();
      float mx = fmaxf(fmaxf(fmaxf(s0.x, s0.y), fmaxf(s0.z, s0.w)), fmaxf(fmaxf(s1.x, s1.y), fmaxf(s1.z, s1.w)));
      mx = x32_max(x16_max(mx));
      if (g == 0) *(LAS float*)(L + L_SM + (r * 4 + w) * 4) = mx;
      arrive(C_S3);
      await(C_S3, 4);
      const f32x4 m0 = LDF4(L_SM + r * 16);
      const float M = fmaxf(fmaxf(m0.x, m0.y), fmaxf(m0.z, m0.w));
      f32x4 p0, p1;
#pragma unroll
      for (int e = 0; e < 4; ++e) { p0[e] = __builtin_amdgcn_exp2f(s0[e] - M); p1[e] = __builtin_amdgcn_exp2f(s1[e] - M); }
      *(LAS v4u*)(L + L_P + w * 1024 + lane * 16) = v4u{pk_bf16(p0.x, p0.y), pk_bf16(p0.z, p0.w), pk_bf16(p1.x, p1.y), pk_bf16(p1.z, p1.w)};
      float ls = ((p0.x + p0.y) + (p0.z + p0.w)) + ((p1.x + p1.y) + (p1.z + p1.w));
      ls = x32_add(x16_add(ls));
      if (g == 0) *(LAS float*)(L + L_SM + 256 + (r * 4 + w) * 4) = ls;
      arrive(C_S4);
      await(C_S4, 4);
      v4u pf[4];
#pragma unroll
      for (int i = 0; i < 4; ++i) pf[i] = LD128(L_P + i * 1024 + lane * 16);
      float osc;
      {
        const f32x4 l0 = LDF4(L_SM + 256 + r * 16), km = LDF4(L_KM);
        const float lse = M + __builtin_amdgcn_logf((l0.x + l0.y) + (l0.z + l0.w));
        const unsigned lw0 = tagw(lse, tag), lw1 = tagw(lse - untag(lw0), tag);
        const unsigned sw = tagw(fmaxf(__builtin_amdgcn_sqrtf(fmaxf(fmaxf(km.x, km.y), fmaxf(km.z, km.w))) * (1.f / 32768.f), 1e-30f), tag);
        osc = __builtin_amdgcn_exp2f((M - untag(lw0)) - untag(lw1)) / untag(sw);
        if (w == 0 && g == 0) STH((v4u{lw0, lw1, sw, sw}), W_LS + (((16 * hg + r) * 2 + ab) * 64 + as) * 16);
      }
      const unsigned tb0 = (tag & 1u) | ((tag & 2u) << 15), tb1 = ((tag >> 2) & 1u) | ((tag & 8u) << 13);
      const int stg = L_KV + w * (32 * 144);
      const unsigned h3base = W_H3 + (((ab * 64 + as) * 2 + hg) * 16 + 4 * w) * 1024 + lane * 16;
      f32x4 oc = {0.f, 0.f, 0.f, 0.f};
#pragma unroll
      for (int i = 0; i < 4; ++i) oc = mfma(vf[i], pf[i], oc);
      __builtin_amdgcn_sched_barrier(0);
#pragma unroll
      for (int ct = 0; ct < 8; ++ct) {
        f32x4 on = {0.f, 0.f, 0.f, 0.f};
        if (ct < 7) {
#pragma unroll
          for (int i = 0; i < 4; ++i) on = mfma(vf[4 * ct + 4 + i], pf[i], on);
        }
        const f32x4 o = oc * osc;
        const unsigned p0 = (pk_f16(o.x, o.y) & 0xfffefffeu) | tb0, p1 = (pk_f16(o.z, o.w) & 0xfffefffeu) | tb1;
        *(LAS v2u*)(L + stg + ((ct >> 2) * 16 + r) * 144 + ((ct & 3) * 4 + g) * 8) = v2u{p0, p1};
        if (ct == 4 || ct == 5) {
          const v4u d = LD128(stg + (8 * (ct - 4) + (lane >> 3)) * 144 + (lane & 7) * 16);
          STH(d, h3base + (ct - 4) * 1024);
        }
        oc = on;
        FENCE();
      }
#pragma unroll
      for (int k = 2; k < 4; ++k) {
        const v4u d = LD128(stg + (8 * k + (lane >> 3)) * 144 + (lane & 7) * 16);
        STH(d, h3base + k * 1024);
      }
    }
    arrive(C_ST);
    __builtin_amdgcn_s_sleep(H3_DELAY);
    {
      const int b = w >> 1;
      const int c8 = lane & 7, sl = 32 * (w & 1) + (lane >> 3);
      v4u v[4] = {};
      v4u lw;
      const unsigned obase = W_H3 + (unsigned)(((b * 64 + sl) * 2 + (h >> 4)) * 16 + 2 * j) * 1024u + (h & 15) * 128u + c8 * 16u;
      const unsigned tb0 = (tag & 1u) | ((tag & 2u) << 15), tb1 = ((tag >> 2) & 1u) | ((tag & 8u) << 13);
      {
        unsigned pend = 0xfu;
        for (int spin = 0; spin < SPIN_CAP; ++spin) {
          asm volatile("" ::: "memory");
          v4u t[4];
          lw = LDH(W_LS + ((h * 2 + b) * 64 + lane) * 16);
#pragma unroll
          for (int i = 0; i < 4; ++i) t[i] = LDH(((pend >> i) & 1u) ? obase + i * (8u * 32768u) : OOB);
          unsigned badm = 0;
#pragma unroll
          for (int i = 0; i < 4; ++i) badm |= ((((t[i].x ^ tb0) | (t[i].y ^ tb1) | (t[i].z ^ tb0) | (t[i].w ^ tb1)) & 0x00010001u) ? 1u : 0u) << i;
          badm |= (unsigned)__builtin_amdgcn_update_dpp(0, (int)badm, 0xb1, 0xf, 0xf, false);
          badm |= (unsigned)__builtin_amdgcn_update_dpp(0, (int)badm, 0x4e, 0xf, 0xf, false);
          badm |= (unsigned)__builtin_amdgcn_update_dpp(0, (int)badm, 0x141, 0xf, 0xf, false);
#pragma unroll
          for (int i = 0; i < 4; ++i) {
            const bool p = (pend >> i) & 1u;
            v[i].x = p ? t[i].x : v[i].x; v[i].y = p ? t[i].y : v[i].y; v[i].z = p ? t[i].z : v[i].z; v[i].w = p ? t[i].w : v[i].w;
          }
          pend &= badm;
          if (__all((pend | bad4(lw, tag)) == 0)) break;
          __builtin_amdgcn_s_sleep(H3_SLEEP);
        }
      }
      const float ls = untag(lw.x);
      float Mx = ls;
      Mx = fmaxf(Mx, dpp_ror<8>(Mx)); Mx = fmaxf(Mx, dpp_ror<4>(Mx)); Mx = fmaxf(Mx, dpp_ror<2>(Mx)); Mx = fmaxf(Mx, dpp_ror<1>(Mx));
      Mx = x32_max(x16_max(Mx));
      const float e = __builtin_amdgcn_exp2f((ls - Mx) + untag(lw.y));
      const float wl = e / wave_sum(e) * untag(lw.z);
      float o8[8] = {0.f, 0.f, 0.f, 0.f, 0.f, 0.f, 0.f, 0.f};
#pragma unroll
      for (int i = 0; i < 4; ++i) {
        const float ws_ = __shfl(wl, sl + 8 * i);
#pragma unroll
        for (int k = 0; k < 4; ++k) {
          const f16x2 hv = __builtin_bit_cast(f16x2, v[i][k] & 0xfffefffeu);
          o8[2 * k] += ws_ * (float)hv.x;
          o8[2 * k + 1] += ws_ * (float)hv.y;
        }
      }
#pragma unroll
      for (int k = 0; k < 8; ++k) { float t = o8[k]; t += dpp_ror<8>(t); o8[k] = x32_add(x16_add(t)); }
      if (lane < 8) {
        *(LAS f32x4*)(L + L_OL + w * 256 + c8 * 32) = f32x4{o8[0], o8[1], o8[2], o8[3]};
        *(LAS f32x4*)(L + L_OL + w * 256 + c8 * 32 + 16) = f32x4{o8[4], o8[5], o8[6], o8[7]};
      }
    }
    arrive(C_S5);
    await(C_S5, 4);
    {
      const int b = r >> 3;
      const f32x4 oa = LDF4(L_OL + (2 * b) * 256 + (16 * w + 4 * g) * 4), ob = LDF4(L_OL + (2 * b + 1) * 256 + (16 * w + 4 * g) * 4);
      const f32x4 ov = oa + ob;
      const unsigned h01 = pk_bf16(ov.x, ov.y), h23 = pk_bf16(ov.z, ov.w);
      const unsigned l01 = pk_bf16(ov.x - bf_lo(h01), ov.y - bf_hi(h01)), l23 = pk_bf16(ov.z - bf_lo(h23), ov.w - bf_hi(h23));
      f32x4 c0 = {0.f, 0.f, 0.f, 0.f}, c1 = {0.f, 0.f, 0.f, 0.f};
      c0 = mfma(buildA(h01, 0), wvv[0], c0); c1 = mfma(buildA(l01, 0), wvv[0], c1);
      c0 = mfma(buildA(h01, 1), wvv[1], c0); c1 = mfma(buildA(l01, 1), wvv[1], c1);
      c0 = mfma(buildA(h23, 0), wvv[2], c0); c1 = mfma(buildA(l23, 0), wvv[2], c1);
      c0 = mfma(buildA(h23, 1), wvv[3], c0); c1 = mfma(buildA(l23, 1), wvv[3], c1);
      *(LAS f32x4*)(L + L_CTX + w * 1024 + (g >> 1) * 512 + (8 * r + 4 * (g & 1)) * 4) = c0 + c1;
    }
    arrive(C_CTX);
    if (xcd == (slot & 7)) {
      v4u q[8] = {};
      const unsigned qbase = W_H4 + (unsigned)(64 * w + (lane >> 3)) * 4096u + slot * 128u + (lane & 7) * 16u;
      __builtin_amdgcn_s_sleep(H4_DELAY);
      GATHER(8, q, qbase, 8u * 4096u);
      f32x4 t4;
#pragma unroll
      for (int e2 = 0; e2 < 4; ++e2) {
        float t = ((untag(q[0][e2]) + untag(q[1][e2])) + (untag(q[2][e2]) + untag(q[3][e2]))) + ((untag(q[4][e2]) + untag(q[5][e2])) + (untag(q[6][e2]) + untag(q[7][e2])));
        t += dpp_ror<8>(t);
        t4[e2] = x32_add(x16_add(t));
      }
      if (lane < 8) *(LAS f32x4*)(L + L_FIN + w * 128 + lane * 16) = t4;
      arrive(C_FIN);
      if (w == 0) {
        await(C_FIN, 4);
        if (lane < 8) {
          const f32x4 o = (LDF4(L_FIN + lane * 16) + LDF4(L_FIN + 128 + lane * 16)) + (LDF4(L_FIN + 256 + lane * 16) + LDF4(L_FIN + 384 + lane * 16));
          *(v2u*)(out + slot * 32 + lane * 4) = v2u{pk_bf16(o.x, o.y), pk_bf16(o.z, o.w)};
          if (bid == 0 && lane == 0) __builtin_amdgcn_raw_buffer_store_b32(epoch + 1u, rs, W_EPOCH, 0, 16);
        }
      }
    }
  } else {
    const int w = wave - 4;
    const u16* src = ckv + ((size_t)ab * 8192 + as * 128 + w * 32) * 512 + lane * 8;
#pragma unroll
    for (int i = 0; i < DMA0; ++i)
      __builtin_amdgcn_global_load_lds((const void*)(src + i * 512), (LAS void*)(L + L_KV + (w * 32 + i) * PITCH), 16, 0, 0);
    await(C_QI, 4);
    const unsigned epoch = __builtin_amdgcn_raw_buffer_load_b32(rs, vzero + W_EPOCH, 0, 16);
#pragma unroll
    for (int i = DMA0; i < 32; ++i)
      __builtin_amdgcn_global_load_lds((const void*)(src + i * 512), (LAS void*)(L + L_KV + (w * 32 + i) * PITCH), 16, 0, 0);
    FENCE();
    arrive(C_ISS);
    __builtin_amdgcn_s_waitcnt(0x0f70);
    arrive(C_KV);
    asm volatile("" ::"v"(cv));
    await(C_GI, 4);
    v4u wov[34];
    const u16* wob = wo + ((size_t)h * 128 + 32 * g) * 512 + 128 * w + 8 * r;
#pragma unroll
    for (int i = 0; i < 4; ++i) wov[i] = ldw(wob + ((i + 4 * j) & 31) * 512);
    const v4u gx0 = ldw(x + lane * 8), gx1 = ldw(x + 512 + lane * 8), gwv = ldw(gw + lane * 8);
    {
      const int u = bid < 224 ? bid : 0;
      const u16* wdb = wd + (size_t)(8 * u + 2 * g) * 512 + 128 * w + 8 * r;
      wov[32] = ldw(wdb);
      wov[33] = ldw(wdb + 512);
    }
    v4u fv[8]; v4u fx;
    const bool ffn1 = (w < 2) && (bid < 224);
    const int cb = bid % 14, rb = bid / 14;
    if (ffn1) {
      const u16* fb = (w == 0 ? wg : wu) + (size_t)(32 * rb + 8 * g) * 1792 + 128 * cb + 8 * r;
#pragma unroll
      for (int i = 0; i < 8; ++i) fv[i] = ldw(fb + (size_t)i * 1792);
      fx = ldw(x + (r >> 3) * 512 + 32 * rb + 8 * g);
    }
    FENCE();
#pragma unroll
    for (int i = 4; i < 32; ++i) wov[i] = ldw(wob + ((i + 4 * j) & 31) * 512);
    FENCE();
    const unsigned tag = (epoch + 1u) & TMASK;
    if (ffn1) {
      f32x4 acc = {0.f, 0.f, 0.f, 0.f};
#pragma unroll
      for (int i = 0; i < 8; ++i) acc = mfma(buildA(fx[i >> 1], i & 1), fv[i], acc);
      STH(tag4(acc, tag), W_HF + (16 * cb + r) * 2048 + ((w * 16 + rb) * 4 + g) * 16);
    }
    float gate0, gate1;
    {
      float s0 = 0.f, s1 = 0.f;
#pragma unroll
      for (int k = 0; k < 4; ++k) {
        s0 += bf_lo(gx0[k]) * bf_lo(gwv[k]) + bf_hi(gx0[k]) * bf_hi(gwv[k]);
        s1 += bf_lo(gx1[k]) * bf_lo(gwv[k]) + bf_hi(gx1[k]) * bf_hi(gwv[k]);
      }
      s0 = wave_sum(s0); s1 = wave_sum(s1);
      gate0 = 1.f / (1.f + __expf(-s0));
      gate1 = 1.f / (1.f + __expf(-s1));
    }
    await(C_ST, 4);
    __builtin_amdgcn_s_sleep(H3_DELAY);
    unsigned hp = 0u, lp = 0u;
    if (bid < 224) {
      v4u ff[2] = {};
      const unsigned fbase = W_HF + bid * 2048 + lane * 16;
      GATHER(2, ff, fbase, 1024u);
      f32x4 hv;
      const float og = 1.f - (((lane >> 1) & 1) ? gate1 : gate0);
#pragma unroll
      for (int e2 = 0; e2 < 4; ++e2) {
        float t = untag(ff[0][e2]); t += dpp_ror<8>(t); t += dpp_ror<4>(t); t = x32_add(x16_add(t));
        float u = untag(ff[1][e2]); u += dpp_ror<8>(u); u += dpp_ror<4>(u); u = x32_add(x16_add(u));
        const float a2 = 2.f * 0.7978845608028654f * (t + 0.044715f * t * t * t);
        hv[e2] = t / (1.f + __expf(-a2)) * u * og;
      }
      const int srcl = (r >> 3) * 2 + (g >> 1);
      const float a0 = __shfl(hv.x, srcl), a1 = __shfl(hv.y, srcl), a2_ = __shfl(hv.z, srcl), a3 = __shfl(hv.w, srcl);
      const float h0 = (g & 1) ? a2_ : a0, h1 = (g & 1) ? a3 : a1;
      hp = pk_bf16(h0, h1);
      lp = pk_bf16(h0 - bf_lo(hp), h1 - bf_hi(hp));
    }
    f32x4 acc = {0.f, 0.f, 0.f, 0.f};
    acc = mfma(buildA(hp, 0), wov[32], acc);
    acc = mfma(buildA(hp, 1), wov[33], acc);
    acc = mfma(buildA(lp, 0), wov[32], acc);
    acc = mfma(buildA(lp, 1), wov[33], acc);
    await(C_CTX, 4);
    {
      const int b = r >> 3;
      const float gb = b ? gate1 : gate0;
      const int off = L_CTX + b * 512 + (32 * g + 4 * jj) * 4;
      const f32x4 cs = ((LDF4(off) + LDF4(off + 1024)) + (LDF4(off + 2048) + LDF4(off + 3072))) * gb;
      *(LAS v2u*)(L + L_PK + w * 512 + b * 256 + (16 * g + 2 * jj) * 4) = v2u{pk_bf16(cs.x, cs.y), pk_bf16(cs.z, cs.w)};
      FENCE();
      v2u pk[8];
#pragma unroll
      for (int k = 0; k < 8; ++k) pk[k] = *(const LAS v2u*)(L + L_PK + w * 512 + b * 256 + (16 * g + ((2 * k + 2 * j) & 15)) * 4);
#pragma unroll
      for (int i = 0; i < 32; ++i) acc = mfma(buildA(pk[i >> 2][(i >> 1) & 1], i & 1), wov[i], acc);
    }
    STH(tag4(acc, tag), W_H4 + bid * 4096 + ((g >> 1) * 128 + 32 * w + 2 * r + (g & 1)) * 16);
  }
}
}

extern "C" void kernel_launch(void* const* d_in, const int* in_sizes, int n_in, void* d_out, int out_size, void* d_ws, size_t ws_size,
                              hipStream_t stream) {
  (void)in_sizes; (void)n_in; (void)out_size; (void)ws_size;
  (void)hipFuncSetAttribute((const void*)mk, hipFuncAttributeMaxDynamicSharedMemorySize, L_END);
  mk<<<256, 512, L_END, stream>>>((const u16*)d_in[0], (const u16*)d_in[1], (const u16*)d_in[2], (const u16*)d_in[3], (const u16*)d_in[4],
                                    (const u16*)d_in[5], (const u16*)d_in[7], (const u16*)d_in[8], (const u16*)d_in[9], (const u16*)d_in[10],
                                    (u16*)d_out, (unsigned char*)d_ws);
}
```
